```python
import torch

def _softmax(x, axis=-1):
    return torch.softmax(x, dim=axis)
def _gelu(x):
    return torch.nn.functional.gelu(x, approximate="tanh")
def _silu(x):
    return torch.nn.functional.silu(x)
def _sigmoid(x):
    return torch.sigmoid(x)
def _layernorm(x, axis=-1, eps=1e-5):
    m = x.mean(dim=axis, keepdim=True)
    v = x.var(dim=axis, keepdim=True, unbiased=False)
    return (x - m) / torch.sqrt(v + eps)
def _scatter_add(tgt, idx, src, axis=0):
    return tgt.clone().index_add_(axis, idx, src)
def _segment_sum(src, seg, n):
    out = torch.zeros((n,) + src.shape[1:], dtype=src.dtype)
    return out.index_add_(0, seg, src)
def _pad(x, pw):
    flat = tuple(v for pair in reversed(pw) for v in pair)
    return torch.nn.functional.pad(x, flat)

kernel_name = 'gated_mix__linear_kernel_hedgehog__dense_activation_gelu__prompt'

def setup_inputs():

    _w = (lambda a: (lambda t: t.to(torch.bfloat16) if t.is_floating_point() else t)(torch.as_tensor(a)))
    def _A_setup():
        B, S, D, H = 1, 2048, 448, 16
        Dh = D // H
        return {
            "x": _w((lambda *s: torch.randn(*s, dtype=torch.bfloat16) * 0.05)(B, S, D)),
            "wqkv": _w((lambda *s: torch.randn(*s, dtype=torch.bfloat16) * 0.8)(D, 3 * D)),
            "w_phi": _w((lambda *s: torch.randn(*s, dtype=torch.bfloat16) * 0.8)(H, Dh, Dh) * (Dh ** -0.5)),
            "wo": _w((lambda *s: torch.randn(*s, dtype=torch.bfloat16) * 0.05)(D, D)),
        }
    _A_in = _A_setup()

    def _B_setup():
        B, S, D, F = 1, 2048, 448, 1792
        return {
            "x": _w((lambda *s: torch.randn(*s, dtype=torch.bfloat16) * 0.05)(B, S, D)),
            "w1": _w((lambda *s: torch.randn(*s, dtype=torch.bfloat16) * 3.2)(D, F) * (D ** -0.5)),
            "b1": _w((lambda *s: torch.randn(*s, dtype=torch.bfloat16) * 3.2)(F) * 0.01),
            "w2": _w((lambda *s: torch.randn(*s, dtype=torch.bfloat16) * 0.2)(F, D) * (F ** -0.5)),
            "b2": _w((lambda *s: torch.randn(*s, dtype=torch.bfloat16) * 0.4)(D) * 0.01),
        }
    _B_in = _B_setup()

    return {
        "A_x": _A_in["x"],
        "A_wqkv": _A_in["wqkv"],
        "A_w_phi": _A_in["w_phi"],
        "A_wo": _A_in["wo"],
        "B_x": _B_in["x"],
        "B_w1": _B_in["w1"],
        "B_b1": _B_in["b1"],
        "B_w2": _B_in["w2"],
        "B_b2": _B_in["b2"],
        "gate_w": _w((lambda *s: torch.randn(*s, dtype=torch.bfloat16) * 0.05)(448) * 16.0),
    }

def reference(A_x, A_wqkv, A_w_phi, A_wo, B_x, B_w1, B_b1, B_w2, B_b2, gate_w):
    def _A_ref(x, wqkv, w_phi, wo):
        B, S, D = x.shape
        H = 16; Dh = D // H
        qkv = (lambda x, *d: x.permute(*d))((x @ wqkv).reshape(B, S, 3, H, Dh), 2, 0, 3, 1, 4)
        q, k, v = qkv[0], qkv[1], qkv[2]
        pq = torch.einsum("bhsd,hdf->bhsf", q, w_phi)
        pk = torch.einsum("bhsd,hdf->bhsf", k, w_phi)
        phi_q = torch.concatenate([_softmax(pq, axis=-1), _softmax(-pq, axis=-1)], axis=-1)
        phi_k = torch.concatenate([_softmax(pk, axis=-1), _softmax(-pk, axis=-1)], axis=-1)
        kv = torch.einsum("bhsf,bhsd->bhfd", phi_k, v)
        z  = torch.einsum("bhsf->bhf", phi_k)
        num = torch.einsum("bhqf,bhfd->bhqd", phi_q, kv)
        den = torch.einsum("bhqf,bhf->bhq", phi_q, z)[..., None] + 1e-6
        o = num / den
        return (lambda x, *d: x.permute(*d))(o, 0, 2, 1, 3).reshape(B, S, D) @ wo

    def _B_ref(x, w1, b1, w2, b2):
        h = torch.einsum("bsd,df->bsf", x, w1) + b1
        h = _gelu(h)
        return torch.einsum("bsf,fd->bsd", h, w2) + b2

    x = A_x
    g = _sigmoid((x * gate_w).sum(-1, keepdims=True))
    a = _A_ref(x, A_wqkv, A_w_phi, A_wo)
    b = _B_ref(x, B_w1, B_b1, B_w2, B_b2)
    return g * a + (1 - g) * b
```

```cpp
#include <hip/hip_runtime.h>
#include <hip/hip_bf16.h>
#include <stdint.h>

typedef unsigned short u16;
static __device__ __forceinline__ float bf2f(u16 v){ return __uint_as_float(((unsigned)v)<<16); }
static __device__ __forceinline__ u16 f2bf(float f){ unsigned u=__float_as_uint(f); u = u + 0x7FFF + ((u>>16)&1); return (u16)(u>>16);}
static __device__ __forceinline__ float rb(float f){ return bf2f(f2bf(f)); }

#define S 2048
#define D 448
#define H 16
#define DH 28
#define F 1792

__global__ void gemm1(const u16* x, const u16* w, float* out, int N, int K){
  int n = blockIdx.x*64 + threadIdx.x; int s = blockIdx.y;
  if (n>=N) return;
  float acc=0; for(int k=0;k<K;k++) acc += bf2f(x[s*K+k])*bf2f(w[k*N+n]);
  out[s*N+n]=acc;
}
__global__ void phi(const float* qkv, const u16* wphi, float* phiq, float* phik){
  int s=blockIdx.x; int t=threadIdx.x; if(t>=32) return; int which=t>>4, h=t&15;
  const float* q = qkv + s*1344 + which*448 + h*28;
  float* o = (which? phik:phiq) + (s*16+h)*56;
  float mx=-1e30f, mn=1e30f;
  for(int f=0;f<28;f++){ float a=0; for(int d=0;d<28;d++) a+= rb(q[d])*bf2f(wphi[(h*28+d)*28+f]); o[f]=a; mx=fmaxf(mx,a); mn=fminf(mn,a);}
  float s1=0,s2=0;
  for(int f=0;f<28;f++){ float a=o[f]; float e1=expf(a-mx), e2=expf(mn-a); o[f]=e1; o[28+f]=e2; s1+=e1; s2+=e2;}
  for(int f=0;f<28;f++){ o[f]=rb(o[f]/s1); o[28+f]=rb(o[28+f]/s2);}
}
__global__ void kvk(const float* qkv, const float* phik, float* kv){
  int h=blockIdx.x, f=blockIdx.y, d=threadIdx.x; if(d>28) return;
  float a=0; for(int s=0;s<S;s++){ float v = d<28? rb(qkv[s*1344+896+h*28+d]) : 1.f; a += phik[(s*16+h)*56+f]*v; }
  kv[(h*56+f)*29+d]=a;
}
__global__ void ok(const float* phiq, const float* kv, u16* o){
  int s=blockIdx.x; int c=threadIdx.x; if(c>=448) return; int h=c/28,d=c%28;
  float num=0,den=0; for(int f=0;f<56;f++){ float p=phiq[(s*16+h)*56+f]; num+=p*rb(kv[(h*56+f)*29+d]); den+=p*rb(kv[(h*56+f)*29+28]); }
  o[s*448+c]=f2bf(num/(den+1e-6f));
}
__global__ void geluk(const float* hin, const u16* b1, u16* h){
  int i=blockIdx.x*256+threadIdx.x; int f=i%F; float v=hin[i]+bf2f(b1[f]);
  float t=tanhf(0.7978845608028654f*(v+0.044715f*v*v*v)); h[i]=f2bf(0.5f*v*(1.f+t));
}
__global__ void fin(const u16* x, const u16* gw, const float* a, const float* b, const u16* b2, u16* out){
  int s=blockIdx.x; int c=threadIdx.x; if(c>=448) return; float g=0; for(int k=0;k<448;k++) g+=bf2f(x[s*448+k])*bf2f(gw[k]);
  g=1.f/(1.f+expf(-g)); out[s*448+c]=f2bf(g*a[s*448+c]+(1.f-g)*(b[s*448+c]+bf2f(b2[c])));
}
extern "C" void kernel_launch(void* const* d_in, const int* in_sizes, int n_in,
                              void* d_out, int out_size, void* d_ws, size_t ws_size,
                              hipStream_t stream) {
  const u16* x=(const u16*)d_in[0]; const u16* wqkv=(const u16*)d_in[1]; const u16* wphi=(const u16*)d_in[2]; const u16* wo=(const u16*)d_in[3];
  const u16* w1=(const u16*)d_in[5]; const u16* b1=(const u16*)d_in[6]; const u16* w2=(const u16*)d_in[7]; const u16* b2=(const u16*)d_in[8]; const u16* gw=(const u16*)d_in[9];
  float* ws=(float*)d_ws;
  float* qkv=ws; float* phiq=qkv+S*1344; float* phik=phiq+S*896; float* kv=phik+S*896; float* hin=kv+16*56*32; float* a=hin+S*F; float* b=a+S*D;
  u16* o=(u16*)(b+S*D); u16* h=o+S*D;
  gemm1<<<dim3(21,S),64,0,stream>>>(x,wqkv,qkv,1344,448);
  phi<<<S,64,0,stream>>>(qkv,wphi,phiq,phik);
  kvk<<<dim3(16,56),64,0,stream>>>(qkv,phik,kv);
  ok<<<S,448,0,stream>>>(phiq,kv,o);
  gemm1<<<dim3(7,S),64,0,stream>>>(o,wo,a,448,448);
  gemm1<<<dim3(28,S),64,0,stream>>>(x,w1,hin,F,448);
  geluk<<<S*F/256,256,0,stream>>>(hin,b1,h);
  gemm1<<<dim3(7,S),64,0,stream>>>(h,w2,b,448,F);
  fin<<<S,448,0,stream>>>(x,gw,a,b,b2,(u16*)d_out);
}
```

```cpp
#include <hip/hip_runtime.h>
#include <stdint.h>

typedef unsigned u32;
typedef unsigned long long u64;
typedef unsigned short u16;
typedef __bf16 bf2 __attribute__((ext_vector_type(2)));
typedef __bf16 bf8 __attribute__((ext_vector_type(8)));
typedef short s4 __attribute__((ext_vector_type(4)));
typedef short s8 __attribute__((ext_vector_type(8)));
typedef float f2 __attribute__((ext_vector_type(2)));
typedef float f4 __attribute__((ext_vector_type(4)));
typedef float f16v __attribute__((ext_vector_type(16)));
typedef u32 u32x2 __attribute__((ext_vector_type(2)));
typedef u32 u32x4 __attribute__((ext_vector_type(4)));
#define LAS __attribute__((address_space(3)))
#define DI __device__ __forceinline__

constexpr u32 WS_EPOCH = 0;
constexpr u32 WS_FK = 0x1000;
constexpr u32 WS_FQ = 0x2000;
constexpr u32 WS_FF = 0x4000;
constexpr u32 WS_FO = 0x5000;
constexpr u32 WS_OMG = 0x6000;
constexpr u32 WS_FH = 0x8000;
constexpr u32 WS_KVF = 0x20000;
constexpr u32 WS_KVP = 0x100000;
constexpr u32 WS_PQ = 0x300000;
constexpr u32 WS_HB = 0x800000;
constexpr u32 WS_OB = 0x1000000;
constexpr u32 WS_DBG = 0x1200000;
constexpr u32 WS_BYTES = 0x1300000;

constexpr u32 L_R0 = 0, L_R1 = 57344;
constexpr u32 L_Z0 = 16384, L_Z1 = 49152, L_Z2 = 81920, L_Z3 = 114688;
constexpr u32 L_WQ = 114688, L_PHK = 143360, L_MISC = 159744, L_V = L_R1;
constexpr u32 L_WPHI = L_MISC, L_GW = L_MISC + 2048, L_B1 = L_MISC + 3072, L_B2 = L_MISC + 3328, L_G = L_MISC + 3584;
constexpr u32 L_RED0 = 0, L_RED1 = L_WQ + 1024;
constexpr u32 LDS_BYTES = 163840;

constexpr u32 SPIN_CAP = 1u << 15;
constexpr float LOG2E = 1.4426950408889634f;

#define MFMA(a, b, c) __builtin_amdgcn_mfma_f32_32x32x16_bf16(__builtin_bit_cast(bf8, a), __builtin_bit_cast(bf8, b), c, 0, 0, 0)
#define SBAR() __builtin_amdgcn_sched_barrier(0)
#define WAITV(N) asm volatile("s_waitcnt vmcnt(%0)" ::"i"(N) : "memory")
#define WAITV_BAR(N) asm volatile("s_waitcnt vmcnt(%0) lgkmcnt(0)\n\ts_barrier" ::"i"(N) : "memory")
#define BAR() asm volatile("s_waitcnt lgkmcnt(0)\n\ts_barrier" ::: "memory")

#define STAMP(k)

DI u32x4 mksrd(const void* p, u32 bytes) {
  u64 a = (u64)p;
  return (u32x4){(u32)a, (u32)(a >> 32) & 0xffffu, bytes, 0x00020000u};
}
DI void dma(u32 m0v, u32 voff, u32x4 srd, u32 soff) {
  asm volatile("s_mov_b32 m0, %0\n\ts_nop 0\n\tbuffer_load_dwordx4 %1, %2, %3 offen lds" ::"s"(m0v), "v"(voff), "s"(srd), "s"(soff) : "memory");
}
template <int OFF> DI void ld16(u32x4& d, u32 voff, u32x4 srd, u32 soff) {
  asm volatile("buffer_load_dwordx4 %0, %1, %2, %3 offen offset:%4 sc1" : "=v"(d) : "v"(voff), "s"(srd), "s"(soff), "i"(OFF) : "memory");
}
DI void ld8(u32x2& d, u32 voff, u32x4 srd, u32 soff) {
  asm volatile("buffer_load_dwordx2 %0, %1, %2, %3 offen sc1" : "=v"(d) : "v"(voff), "s"(srd), "s"(soff) : "memory");
}
DI void ld4(u32& d, u32 voff, u32x4 srd, u32 soff) {
  asm volatile("buffer_load_dword %0, %1, %2, %3 offen sc1" : "=v"(d) : "v"(voff), "s"(srd), "s"(soff) : "memory");
}
template <int OFF> DI void st16(u32x4 d, u32 voff, u32x4 srd, u32 soff) {
  asm volatile("buffer_store_dwordx4 %0, %1, %2, %3 offen offset:%4 sc1\n\ts_nop 1" ::"v"(d), "v"(voff), "s"(srd), "s"(soff), "i"(OFF) : "memory");
}
DI void st8(u32x2 d, u32 voff, u32x4 srd, u32 soff) {
  asm volatile("buffer_store_dwordx2 %0, %1, %2, %3 offen sc1\n\ts_nop 1" ::"v"(d), "v"(voff), "s"(srd), "s"(soff) : "memory");
}
DI void st4(u32 d, u32 voff, u32x4 srd, u32 soff) {
  asm volatile("buffer_store_dword %0, %1, %2, %3 offen sc1\n\ts_nop 1" ::"v"(d), "v"(voff), "s"(srd), "s"(soff) : "memory");
}
template <int N> DI void waitr(u32x4& a) { asm volatile("s_waitcnt vmcnt(%1)" : "+v"(a) : "i"(N) : "memory"); }
template <int N> DI void waitr(u32x2& a) { asm volatile("s_waitcnt vmcnt(%1)" : "+v"(a) : "i"(N) : "memory"); }
DI void pin(u32x4& a) { asm volatile("" : "+v"(a)); }

DI void flag(u32x2 tagv, u32 flane, u32x4 srd, u32 soff) { st8(tagv, flane, srd, soff); }
DI u64 tagmask(u32x2 p, u32x2 tagv) { return __ballot(p.x == tagv.x && p.y == tagv.y); }
DI u64 spin(u64 m, u64 need, u32 voff, u32x4 srd, u32 soff, u32x2 tagv) {
  for (u32 it = 0; (m & need) != need && it < SPIN_CAP; ++it) {
    u32x2 p;
    ld8(p, voff, srd, soff);
    waitr<0>(p);
    m = tagmask(p, tagv);
  }
  return m;
}

DI u32 pk(float a, float b) { return __builtin_bit_cast(u32, __builtin_convertvector((f2){a, b}, bf2)); }
DI u32x4 pk8(const f16v& v, int o) { return (u32x4){pk(v[o], v[o + 1]), pk(v[o + 2], v[o + 3]), pk(v[o + 4], v[o + 5]), pk(v[o + 6], v[o + 7])}; }
DI u32x2 trd(LAS char* p) { return __builtin_bit_cast(u32x2, __builtin_amdgcn_ds_read_tr16_b64_v4i16((LAS s4*)p)); }
DI u32x4 tr2(LAS char* p, LAS char* q) { u32x2 a = trd(p), b = trd(q); return (u32x4){a.x, a.y, b.x, b.y}; }
DI float blo(u32 v) { return __uint_as_float(v << 16); }
DI float bhi(u32 v) { return __uint_as_float(v & 0xffff0000u); }
DI float xsum(float v) { auto r = __builtin_amdgcn_permlane32_swap(__float_as_uint(v), __float_as_uint(v), false, false); return __uint_as_float(r[0]) + __uint_as_float(r[1]); }
DI float xhi(float v) { auto r = __builtin_amdgcn_permlane32_swap(__float_as_uint(v), __float_as_uint(v), false, false); return __uint_as_float(r[1]); }
DI float mx3(float a, float b, float c) { return __builtin_fmaxf(__builtin_fmaxf(a, b), c); }
DI float mn3(float a, float b, float c) { return __builtin_fminf(__builtin_fminf(a, b), c); }
DI float dot2(u32 a, u32 b, float c) { return __builtin_amdgcn_fdot2_f32_bf16(__builtin_bit_cast(bf2, a), __builtin_bit_cast(bf2, b), c, false); }

DI f16v bias16(LAS char* p) {
  f16v a;
#pragma unroll
  for (int g = 0; g < 4; ++g) {
    u32x2 v = *(LAS u32x2*)(p + 16 * g);
    a[4 * g] = blo(v.x); a[4 * g + 1] = bhi(v.x); a[4 * g + 2] = blo(v.y); a[4 * g + 3] = bhi(v.y);
  }
  return a;
}
DI float gelu1(float x, float cg) {
  const float C0 = -2.0f * 0.7978845608028654f * LOG2E, C1 = C0 * 0.044715f;
  float t = __builtin_fmaf(x * x, C1, C0);
  float e = __builtin_amdgcn_exp2f(x * t);
  return x * __builtin_amdgcn_rcpf(__builtin_fmaf(e, cg, cg));
}
DI void featmap(const f16v& a, LAS char* wp, bool hi, f16v& ep, f16v& en) {
  u32x4 w0 = tr2(wp, wp + 8 * 56);
  u32x4 w1 = tr2(wp + 16 * 56, wp + 24 * 56);
  w1.z = hi ? 0u : w1.z; w1.w = hi ? 0u : w1.w;
  f16v p = {};
  p = MFMA(w0, pk8(a, 0), p);
  p = MFMA(w1, pk8(a, 8), p);
  float mx = mx3(p[0], p[1], p[2]), mn = mn3(p[0], p[1], p[2]);
#pragma unroll
  for (int i = 3; i < 15; i += 2) { mx = mx3(mx, p[i], p[i + 1]); mn = mn3(mn, p[i], p[i + 1]); }
  mx = __builtin_fmaxf(mx, p[15]); mn = __builtin_fminf(mn, p[15]);
  { auto r = __builtin_amdgcn_permlane32_swap(__float_as_uint(mx), __float_as_uint(mx), false, false); mx = __builtin_fmaxf(__uint_as_float(r[0]), __uint_as_float(r[1])); }
  { auto r = __builtin_amdgcn_permlane32_swap(__float_as_uint(mn), __float_as_uint(mn), false, false); mn = __builtin_fminf(__uint_as_float(r[0]), __uint_as_float(r[1])); }
  const float a0 = -mx * LOG2E, a1 = mn * LOG2E;
#pragma unroll
  for (int i = 0; i < 16; ++i) {
    ep[i] = __builtin_amdgcn_exp2f(__builtin_fmaf(p[i], LOG2E, a0));
    en[i] = __builtin_amdgcn_exp2f(__builtin_fmaf(p[i], -LOG2E, a1));
  }
}
DI float sum14(const f16v& e, bool hi) {
  float a = (e[0] + e[1]) + (e[2] + e[3]), b = (e[4] + e[5]) + (e[6] + e[7]), c = (e[8] + e[9]) + (e[10] + e[11]), d = (e[12] + e[13]) + (e[14] + e[15]);
  return xsum((a + b) + (c + (hi ? 0.f : d)));
}

__global__ __launch_bounds__(512, 2) void fused_kernel(const u16* __restrict__ x, const u16* __restrict__ wqkv, const u16* __restrict__ wphi,
                                                       const u16* __restrict__ wo, const u16* __restrict__ w1, const u16* __restrict__ b1,
                                                       const u16* __restrict__ w2, const u16* __restrict__ b2, const u16* __restrict__ gw,
                                                       u16* __restrict__ out, char* __restrict__ ws) {
  __shared__ __attribute__((aligned(16))) char smem_[LDS_BYTES];
  LAS char* sm = (LAS char*)smem_;
  const u32 lds0 = (u32)(size_t)sm;
  STAMP(0);
  const u32 tid = threadIdx.x, lane = tid & 63;
  const u32 w = __builtin_amdgcn_readfirstlane(tid >> 6), ch = w >> 2, tg = w & 3;
  const u32 b = blockIdx.x, xcd = b & 7, rr = b >> 3;
  const u32 t = (xcd & 1) * 8 + (rr & 7), hd = (xcd >> 1) * 4 + (rr >> 3);
  const bool special = ((hd & 3) == 3) && (((hd >> 2) & 1) == (t & 1));
  const u32 js = special ? 0u : ((t & 1) ? hd - (hd > 7) : hd - (hd > 3) - (hd > 11));
  const u32 nu = rr < 24 ? rr : 24 + ((rr & 7) >> 1);
  const u32 ut = xcd * 4 + (nu & 3), ct = nu >> 2;
  const u32 r = lane & 31, h = lane >> 5, grp = (lane >> 4) & 1, q = (lane >> 2) & 3, p = lane & 3;
  const bool hi = h != 0;

  const u32x4 sX = mksrd(x, 2048 * 896), sQKV = mksrd(wqkv, 448 * 2688), sW1 = mksrd(w1, special ? 0u : 448u * 3584u), sWS = mksrd(ws, WS_BYTES);

  u32x2 tagv;
  ld8(tagv, 0u, sWS, WS_EPOCH);
  if (w < 4) {
    const char* g;
    if (w == 0) g = (const char*)wphi + hd * 1568 + lane * 16;
    else if (w == 1) g = (const char*)wphi + hd * 1568 + 1024 + (lane < 33 ? lane : 33) * 16;
    else if (w == 2) g = (const char*)gw + (lane < 55 ? lane : 55) * 16;
    else g = (lane >= 16 && lane < 24) ? (const char*)b2 + ct * 128 + (lane - 16) * 16 : (const char*)b1 + js * 256 + (lane & 15) * 16;
    if (w < 3 || lane < 32)
      asm volatile("s_mov_b32 m0, %0\n\ts_nop 0\n\tglobal_load_lds_dwordx4 %1, off" ::"s"(lds0 + L_MISC + w * 1024), "v"(g) : "memory");
  }
  const u32 l8 = lane >> 3, c8 = lane & 7;
  const u32 vx0 = (t * 128 + 16 * w + l8) * 896 + ((c8 ^ (lane >> 4)) << 4);
  const u32 vx1 = (t * 128 + 16 * w + 8 + l8) * 896 + ((c8 ^ (4 + (lane >> 4))) << 4);
  const u32 c4 = p == 3 ? 40u : p * 16u;
  const u32 vq = (lane >> 2) * 2688 + hd * 56 + c4;
  const u32 vkv = vq + 16 * tg * 2688 + 896 * (1 + ch);
  const u32 sw8 = (c8 ^ ((l8 & 2) << 1)) << 4;
  const u32 vw1 = (8 * w + l8) * 3584 + js * 256 + sw8;
  const u32 mw = lds0 + w * 1024;

#define CHUNK(c, Z)                                           \
  do {                                                        \
    dma(lds0 + (Z) + w * 2048, vx0, sX, (c) * 128);           \
    dma(lds0 + (Z) + w * 2048 + 1024, vx1, sX, (c) * 128);    \
    dma(mw + (Z) + 16384, vkv, sQKV, (c) * 64 * 2688);        \
    dma(mw + (Z) + 24576, vw1, sW1, (c) * 64 * 3584);         \
  } while (0)
#define L1U(u) dma(mw + L_R0 + (u) * 8192, vw1, sW1, (u) * 64 * 3584 + 128)
  CHUNK(0, L_Z0); CHUNK(1, L_Z1); CHUNK(2, L_Z2); CHUNK(3, L_Z3);

  const u32 ax = (tg * 32 + r) * 128 + ((h ^ ((r >> 1) & 7)) << 4);
  const u32 sl = 4 * grp + p;
  const u32 a64 = (8 * h + q) * 64 + ((sl ^ (sl >= 6 ? 1u : 0u)) << 3);
  const u32 a128 = (8 * h + q) * 128 + ((ch ^ (q >> 1)) << 6) + grp * 32 + p * 8;
  const u32 flane = lane == 0 ? 0u : 0x80000000u;

  u32x4 xf[28];
  f16v accA = {}, accH, accH1;
  float gl = 0.f;
#define STEP(c, Z)                                                                                   \
  do {                                                                                               \
    _Pragma("unroll") for (int s = 0; s < 4; ++s) {                                                  \
      xf[4 * (c) + s] = *(LAS u32x4*)(sm + (Z) + (ax ^ (32 * s)));                                   \
      u32x4 gf = *(LAS u32x4*)(sm + L_GW + (c) * 128 + s * 32 + h * 16);                             \
      LAS char* pa = sm + (Z) + 16384 + ch * 4096 + s * 1024 + a64;                                  \
      LAS char* ph = sm + (Z) + 24576 + s * 2048 + a128;                                             \
      u32x4 wa = tr2(pa, pa + 256), wh = tr2(ph, ph + 512);                                          \
      accA = MFMA(wa, xf[4 * (c) + s], accA);                                                        \
      accH = MFMA(wh, xf[4 * (c) + s], accH);                                                        \
      _Pragma("unroll") for (int e = 0; e < 4; ++e) gl = dot2(xf[4 * (c) + s][e], gf[e], gl);        \
    }                                                                                                \
  } while (0)

  waitr<12>(tagv);
  BAR();
  STAMP(1);
  {
    u64 e = ((u64)__builtin_amdgcn_readfirstlane(tagv.y) << 32 | __builtin_amdgcn_readfirstlane(tagv.x)) + 0x9E3779B97F4A7C15ull;
    tagv = (u32x2){(u32)e, (u32)(e >> 32)};
  }
  accH = bias16(sm + L_B1 + ch * 64 + h * 8);
  STEP(0, L_Z0);
  WAITV_BAR(8); CHUNK(4, L_Z0); STEP(1, L_Z1);
  WAITV_BAR(8); CHUNK(5, L_Z1); STEP(2, L_Z2);
  WAITV_BAR(8); CHUNK(6, L_Z2); STEP(3, L_Z3);
  WAITV_BAR(8);
#pragma unroll
  for (int n = 0; n < 3; ++n) dma(mw + L_WQ + n * 8192, vq, sQKV, (n * 8 + w) * 16 * 2688);
  if (ch == 0) dma(mw + L_WQ + 3 * 8192, vq, sQKV, (24 + w) * 16 * 2688);
  STEP(4, L_Z0);
  WAITV_BAR(7); L1U(0); L1U(1); L1U(2); L1U(3); STEP(5, L_Z1);
  WAITV_BAR(7); L1U(4); L1U(5); L1U(6); STEP(6, L_Z2);
  STAMP(2);
  accH1 = bias16(sm + L_B1 + 128 + ch * 64 + h * 8);
  gl = xsum(gl);
  const float cg = __builtin_fminf(1.f + __builtin_amdgcn_exp2f(gl * LOG2E), 3.0e38f);

  const u32x4 sW2 = mksrd(w2, 1792 * 896);
  const u32 vb = l8 * 896 + ct * 128 + sw8;
  u32 rowoff[7];
#pragma unroll
  for (int u = 0; u < 7; ++u) { u32 m = 7 * tg + u; rowoff[u] = (128 * (m >> 1) + 16 * (m & 1)) * 896; }
  const u32 mb = lds0 + tg * 2048;
  const u32 l16 = lane * 16;
  const u32 atx = (tg * 32 + r) * 64 + ((h ^ ((r >> 1) & 7)) << 3);
  LAS char* wp = sm + L_WPHI + (4 * h + q) * 56 + (sl == 7 ? 48u : sl * 8);
  const u32 hbP = WS_HB + ((t * 4 + tg) * 4 * 28 + 2 * js) * 1024;
  const u32 fhP = WS_FH + (t * 4 + tg) * 512 + js * 8;

  if (ch == 0) {
    f16v ep, en;
    featmap(accA, wp, hi, ep, en);
    const float rp = __builtin_amdgcn_rcpf(sum14(ep, hi)), rn = __builtin_amdgcn_rcpf(sum14(en, hi));
#pragma unroll
    for (int g = 0; g < 4; ++g) {
      *(LAS u32x2*)(sm + L_PHK + (atx ^ (16 * g))) = (u32x2){pk(ep[4 * g] * rp, ep[4 * g + 1] * rp), pk(ep[4 * g + 2] * rp, ep[4 * g + 3] * rp)};
      *(LAS u32x2*)(sm + L_PHK + 8192 + (atx ^ (16 * g))) = (u32x2){pk(en[4 * g] * rn, en[4 * g + 1] * rn), pk(en[4 * g + 2] * rn, en[4 * g + 3] * rn)};
    }
    if (!hi) *(LAS float*)(sm + L_G + (tg * 32 + r) * 4) = gl;
    WAITV_BAR(7);
    STAMP(3);
    if (tg < 2) {
      const u32 a0 = (8 * h + q) * 64 + ((sl ^ (4 * h + (q >> 1))) << 3), a1 = (a0 ^ 16) + 256;
      f16v kv = {};
#pragma unroll
      for (int ks = 0; ks < 8; ++ks) {
        u32x4 fa = tr2(sm + L_PHK + tg * 8192 + ks * 1024 + a0, sm + L_PHK + tg * 8192 + ks * 1024 + a1);
        u32x4 fb = tr2(sm + L_V + ks * 1024 + a0, sm + L_V + ks * 1024 + a1);
        kv = MFMA(fa, fb, kv);
      }
      u32x4 o0 = __builtin_bit_cast(u32x4, (f4){kv[0], kv[1], kv[2], kv[3]}), o1 = __builtin_bit_cast(u32x4, (f4){kv[4], kv[5], kv[6], kv[7]});
      u32x4 o2 = __builtin_bit_cast(u32x4, (f4){kv[8], kv[9], kv[10], kv[11]}), o3 = __builtin_bit_cast(u32x4, (f4){kv[12], kv[13], kv[14], kv[15]});
      asm volatile("s_nop 7\n\ts_nop 4" : "+v"(o0), "+v"(o1), "+v"(o2), "+v"(o3));
      const u32 so = WS_KVP + ((hd * 2 + tg) * 16 + t) * 4096;
      st16<0>(o0, l16, sWS, so); st16<1024>(o1, l16, sWS, so); st16<2048>(o2, l16, sWS, so); st16<3072>(o3, l16, sWS, so);
    }
  } else {
    accA[12] = hi ? 1.0f : accA[12];
#pragma unroll
    for (int g = 0; g < 4; ++g)
      *(LAS u32x2*)(sm + L_V + (atx ^ (16 * g))) = (u32x2){pk(accA[4 * g], accA[4 * g + 1]), pk(accA[4 * g + 2], accA[4 * g + 3])};
  }
  if (!special) {
    const u32 vb0 = vb + 32 * 896, vb1 = vb0 + 8 * 896;
#pragma unroll
    for (int g = 0; g < 4; ++g) {
      if (ch == 0) {
#pragma unroll
        for (int n = 3 * g; n < 3 * g + 3; ++n) dma(mb + L_R1 + (1 + (n >> 1)) * 8192 + (n & 1) * 1024, (n & 1) ? vb1 : vb0, sW2, rowoff[1 + (n >> 1)]);
      }
#pragma unroll
      for (int e = 0; e < 4; ++e) accH[4 * g + e] = gelu1(accH[4 * g + e], cg);
      SBAR();
    }
    const u32 so = hbP + (1 - ch) * 28 * 1024;
    st16<0>(pk8(accH, 0), l16, sWS, so);
    st16<1024>(pk8(accH, 8), l16, sWS, so);
  }
  if (ch == 0) {
    if (special) WAITV(0); else WAITV(14);
    if (tg < 2) flag(tagv, flane, sWS, WS_FK + ((hd * 2 + tg) * 16 + t) * 8);
    if (!special) BAR();
    STAMP(4);
  } else {
    if (special) WAITV_BAR(7); else WAITV_BAR(9);
    STAMP(3);
    f16v aq = {};
#pragma unroll
    for (int s = 0; s < 28; ++s) {
      LAS char* pa = sm + L_WQ + s * 1024 + a64;
      aq = MFMA(tr2(pa, pa + 256), xf[s], aq);
    }
    if (!special) {
      WAITV(0);
      flag(tagv, flane, sWS, fhP);
      BAR();
      STAMP(4);
      dma(mb + L_R1, vb + 32 * 896, sW2, rowoff[0]);
      dma(mb + L_R1 + 1024, vb + 40 * 896, sW2, rowoff[0]);
    }
    f16v ep, en;
    featmap(aq, wp, hi, ep, en);
    const u32 so = WS_PQ + ((t * 16 + hd) * 4 + tg) * 4096;
    st16<0>(pk8(ep, 0), l16, sWS, so); st16<1024>(pk8(ep, 8), l16, sWS, so);
    st16<2048>(pk8(en, 0), l16, sWS, so); st16<3072>(pk8(en, 8), l16, sWS, so);
    if (!special) BAR();
    else { WAITV(0); flag(tagv, flane, sWS, WS_FQ + ((t * 16 + hd) * 4 + tg) * 8); }
  }

  if (special) {
    const u32 half = hd >> 3;
    if (ch == 0) {
      spin(0, 0xffffull, (lane & 15) * 8, sWS, WS_FK + (t * 2 + half) * 128, tagv);
      STAMP(5);
      u32x4 v[16];
      const u32 so = WS_KVP + (t * 2 + half) * 65536 + tg * 1024;
#pragma unroll
      for (int k = 0; k < 16; ++k) ld16<0>(v[k], l16, sWS, so + k * 4096);
      waitr<0>(v[0]);
#pragma unroll
      for (int k = 1; k < 16; ++k) pin(v[k]);
      f4 a = __builtin_bit_cast(f4, v[0]);
#pragma unroll
      for (int k = 1; k < 16; ++k) a += __builtin_bit_cast(f4, v[k]);
      u32x2 o = {pk(a[0], a[1]), pk(a[2], a[3])};
      if (r == 29) o = (u32x2){0x3f803f80u, 0x3f803f80u};
      if (hi && tg == 3) o = (u32x2){0u, 0u};
      st8(o, l16, sWS, WS_KVF + (t * 2 + half) * 2048 + (tg >> 1) * 1024 + (tg & 1) * 8);
      WAITV(0);
      flag(tagv, flane, sWS, WS_FF + t * 128 + (half * 4 + tg) * 8);
      STAMP(6);
    }
    const u32 oh = half * 8 + w;
    spin(0, 0xfull, (lane & 3) * 8, sWS, WS_FQ + (t * 16 + oh) * 32, tagv);
    u32x4 e[16];
    {
      const u32 so = WS_PQ + (t * 16 + oh) * 16384;
#pragma unroll
      for (int k = 0; k < 4; ++k) {
        ld16<0>(e[4 * k], l16, sWS, so + k * 4096); ld16<1024>(e[4 * k + 1], l16, sWS, so + k * 4096);
        ld16<2048>(e[4 * k + 2], l16, sWS, so + k * 4096); ld16<3072>(e[4 * k + 3], l16, sWS, so + k * 4096);
      }
    }
    WAITV(0);
#pragma unroll
    for (int k = 0; k < 16; ++k) pin(e[k]);
    STAMP(7);
    spin(0, 0xffull, (lane & 7) * 8, sWS, WS_FF + oh * 128, tagv);
    STAMP(8);
    u32x4 kf[4];
    ld16<0>(kf[0], l16, sWS, WS_KVF + oh * 4096); ld16<1024>(kf[1], l16, sWS, WS_KVF + oh * 4096);
    ld16<2048>(kf[2], l16, sWS, WS_KVF + oh * 4096); ld16<3072>(kf[3], l16, sWS, WS_KVF + oh * 4096);
    if (half == 0 && ch == 0 && !hi) st4(__float_as_uint(__builtin_amdgcn_rcpf(cg)), r * 4, sWS, WS_OMG + (t * 128 + tg * 32) * 4);
    WAITV(0);
    pin(kf[0]); pin(kf[1]); pin(kf[2]); pin(kf[3]);
#pragma unroll
    for (int k = 0; k < 4; ++k) {
      f16v ap = {}, an = {};
      ap = MFMA(kf[0], e[4 * k], ap); an = MFMA(kf[2], e[4 * k + 2], an);
      ap = MFMA(kf[1], e[4 * k + 1], ap); an = MFMA(kf[3], e[4 * k + 3], an);
      const float lg = *(LAS float*)(sm + L_G + (k * 32 + r) * 4);
      const float g = __builtin_amdgcn_rcpf(1.f + __builtin_amdgcn_exp2f(-lg * LOG2E));
      const float ip = __builtin_amdgcn_rcpf(ap[13]), in = __builtin_amdgcn_rcpf(an[13]);
      const float sc = g * __builtin_amdgcn_rcpf(ap[12] * ip + an[12] * in + 1e-6f);
      const float al = xhi(sc * ip), be = xhi(sc * in);
      f16v o;
#pragma unroll
      for (int i = 0; i < 16; ++i) o[i] = ap[i] * al + an[i] * be;
      u32x4 o0 = pk8(o, 0), o1 = pk8(o, 8);
      o1.z = hi ? 0u : o1.z; o1.w = hi ? 0u : o1.w;
      const u32 so = WS_OB + ((t * 4 + k) * 16 + oh) * 2048;
      st16<0>(o0, l16, sWS, so); st16<1024>(o1, l16, sWS, so);
    }
    WAITV(0);
    flag(tagv, flane, sWS, WS_FO + (t * 16 + oh) * 8);
    STAMP(9);
  } else {
    const u32 tgG = ut * 2 + ch;
    const u32 fhC = WS_FH + tgG * 512;
    u32x2 pl;
#pragma unroll
    for (int s = 0; s < 28; ++s) {
      LAS char* ph = sm + L_R0 + s * 2048 + a128;
      accH1 = MFMA(tr2(ph, ph + 512), xf[s], accH1);
      if (s == 18) { SBAR(); ld8(pl, lane * 8, sWS, fhC); SBAR(); }
    }
    waitr<0>(pl);
    flag(tagv, flane, sWS, ch == 0 ? fhP + 128 : WS_FQ + ((t * 16 + hd) * 4 + tg) * 8);
    BAR();
    STAMP(5);
    u64 fm = spin(tagmask(pl, tagv), 0x3fffull, lane * 8, sWS, fhC, tagv);
    u32x4 A0[7], A1[7], A2[7], A3[7], A4[8];
    const u32 hbC = WS_HB + (tgG * 4 * 28 + 7 * tg) * 1024;
#define LOADA(A, so)                                                                                                    \
  do {                                                                                                                  \
    ld16<0>(A[0], l16, sWS, (so)); ld16<1024>(A[1], l16, sWS, (so)); ld16<2048>(A[2], l16, sWS, (so));                  \
    ld16<3072>(A[3], l16, sWS, (so)); ld16<0>(A[4], l16, sWS, (so) + 4096); ld16<1024>(A[5], l16, sWS, (so) + 4096);    \
    ld16<2048>(A[6], l16, sWS, (so) + 4096);                                                                            \
  } while (0)
    LOADA(A0, hbC);
    STAMP(6);
#pragma unroll
    for (int e = 0; e < 16; ++e) accH1[e] = gelu1(accH1[e], cg);
    {
      const u32 so = hbP + (2 + ch) * 28 * 1024;
      st16<0>(pk8(accH1, 0), l16, sWS, so);
      st16<1024>(pk8(accH1, 8), l16, sWS, so);
    }
    if (ch == 0) {
      BAR();
      ld8(pl, lane * 8, sWS, fhC);
#pragma unroll
      for (int n = 0; n < 14; ++n) dma(mb + L_R0 + (n >> 1) * 8192 + (n & 1) * 1024, (n & 1) ? vb + 8 * 896 : vb, sW2, rowoff[n >> 1]);
      waitr<14>(pl);
      flag(tagv, flane, sWS, fhP + 256);
      fm = tagmask(pl, tagv);
    }
    fm = spin(fm, 0x3fffull << 16, lane * 8, sWS, fhC, tagv);
    LOADA(A1, hbC + 28 * 1024);
    STAMP(7);
    f16v acc0 = {}, acc1 = {};
    const u32 ab0 = tg * 2048 + (4 * h + q) * 128 + ((q >> 1) << 6) + grp * 32 + p * 8, ab1 = ab0 ^ 64;
#define SEG(A, R, N0)                                                   \
  do {                                                                  \
    _Pragma("unroll") for (int u = 0; u < 7; ++u) {                     \
      LAS char* p0 = sm + (R) + u * 8192 + ab0;                         \
      LAS char* p1 = sm + (R) + u * 8192 + ab1;                         \
      u32x4 f0 = tr2(p0, p0 + 1024), f1 = tr2(p1, p1 + 1024);           \
      asm volatile("s_waitcnt vmcnt(%1)" : "+v"(A[u]) : "i"((N0) + 6 - u)); \
      acc0 = MFMA(f0, A[u], acc0);                                      \
      acc1 = MFMA(f1, A[u], acc1);                                      \
    }                                                                   \
  } while (0)
    const u32 vbh = vb + ch * 8 * 896, mbh = mb + ch * 1024;
#define STAGEB(R, so) _Pragma("unroll") for (int u = 0; u < 7; ++u) dma(mbh + (R) + u * 8192, vbh + (so) * 896, sW2, rowoff[u])
    SEG(A0, L_R1, 9);
    WAITV(7);
    if (ch == 1) flag(tagv, flane, sWS, fhP + 384);
    BAR();
    STAMP(8);
    ld8(pl, lane * 8, sWS, fhC);
    STAGEB(L_R1, 64);
    waitr<7>(pl);
    fm = spin(tagmask(pl, tagv), 0x3fffull << 32, lane * 8, sWS, fhC, tagv);
    LOADA(A2, hbC + 2 * 28 * 1024);
    SEG(A1, L_R0, 14);
    WAITV_BAR(7);
    STAMP(9);
    ld8(pl, lane * 8, sWS, fhC);
    STAGEB(L_R0, 96);
    SEG(A2, L_R1, 8);
    waitr<7>(pl);
    fm = spin(tagmask(pl, tagv), 0x3fffull << 48, lane * 8, sWS, fhC, tagv);
    LOADA(A3, hbC + 3 * 28 * 1024);
    WAITV_BAR(7);
    STAMP(10);
    const u32 foC = WS_FO + (ut >> 1) * 128;
    ld8(pl, (lane & 15) * 8, sWS, foC);
    {
      const u32x4 sWO = mksrd(wo, 448 * 896);
#pragma unroll
      for (int u = 0; u < 7; ++u) dma(lds0 + L_R1 + tg * 14336 + ch * 1024 + u * 2048, vbh, sWO, (tg * 112 + u * 16) * 896);
    }
    SEG(A3, L_R0, 8);
    waitr<7>(pl);
    spin(tagmask(pl, tagv), 0xffffull, (lane & 15) * 8, sWS, foC, tagv);
    STAMP(11);
    u32 omg;
    ld4(omg, r * 4, sWS, WS_OMG + tgG * 128);
    {
      const u32 so = WS_OB + (tgG * 16 + 4 * tg) * 2048;
      LOADA(A4, so);
      ld16<3072>(A4[7], l16, sWS, so + 4096);
    }
    WAITV_BAR(9);
    STAMP(12);
    {
      const u32 ao0 = ab0 + tg * (14336 - 2048), ao1 = ao0 ^ 64;
#pragma unroll
      for (int u = 0; u < 8; ++u) {
        LAS char* p0 = sm + L_R1 + (28 * (u >> 1) + 16 * (u & 1)) * 128 + ao0;
        LAS char* p1 = sm + L_R1 + (28 * (u >> 1) + 16 * (u & 1)) * 128 + ao1;
        u32x4 f0 = tr2(p0, p0 + 1024), f1 = tr2(p1, p1 + 1024);
        asm volatile("s_waitcnt vmcnt(%1)" : "+v"(A4[u]) : "i"(7 - u));
        acc0 = MFMA(f0, A4[u], acc0);
        acc1 = MFMA(f1, A4[u], acc1);
      }
    }
    asm volatile("" : "+v"(omg));
    STAMP(13);
    LAS char* red = sm + (ch ? L_RED1 : L_RED0) + l16;
#pragma unroll
    for (int g = 0; g < 4; ++g) {
      *(LAS f4*)(red + (g >> 1) * 8192 + tg * 2048 + (g & 1) * 1024) = (f4){acc0[4 * g], acc0[4 * g + 1], acc0[4 * g + 2], acc0[4 * g + 3]};
      *(LAS f4*)(red + (2 + (g >> 1)) * 8192 + tg * 2048 + (g & 1) * 1024) = (f4){acc1[4 * g], acc1[4 * g + 1], acc1[4 * g + 2], acc1[4 * g + 3]};
    }
    const u32x2 bA = *(LAS u32x2*)(sm + L_B2 + tg * 32 + h * 8), bB = *(LAS u32x2*)(sm + L_B2 + tg * 32 + 16 + h * 8);
    BAR();
    const float om = __uint_as_float(omg);
    f4 sa = (f4){blo(bA.x), bhi(bA.x), blo(bA.y), bhi(bA.y)} * om, sb = (f4){blo(bB.x), bhi(bB.x), blo(bB.y), bhi(bB.y)} * om;
#pragma unroll
    for (int k = 0; k < 4; ++k) {
      sa += *(LAS f4*)(red + tg * 8192 + k * 2048);
      sb += *(LAS f4*)(red + tg * 8192 + k * 2048 + 1024);
    }
    auto s0 = __builtin_amdgcn_permlane32_swap(pk(sa[0], sa[1]), pk(sb[0], sb[1]), false, false);
    auto s1 = __builtin_amdgcn_permlane32_swap(pk(sa[2], sa[3]), pk(sb[2], sb[3]), false, false);
    *(u32x4*)((char*)out + (tgG * 32 + r) * 896 + ct * 128 + tg * 32 + h * 16) = (u32x4){s0[0], s1[0], s0[1], s1[1]};
    if (b == 0 && w == 0) flag(tagv, flane, sWS, WS_EPOCH);
    STAMP(14);
  }
}

extern "C" void kernel_launch(void* const* d_in, const int* in_sizes, int n_in, void* d_out, int out_size, void* d_ws, size_t ws_size,
                              hipStream_t stream) {
  fused_kernel<<<256, 512, 0, stream>>>((const u16*)d_in[0], (const u16*)d_in[1], (const u16*)d_in[2], (const u16*)d_in[3], (const u16*)d_in[5],
                                        (const u16*)d_in[6], (const u16*)d_in[7], (const u16*)d_in[8], (const u16*)d_in[9], (u16*)d_out,
                                        (char*)d_ws);
}
```

```cpp
#include <hip/hip_runtime.h>
#include <stdint.h>
#define STAMPS 1

typedef unsigned u32;
typedef unsigned long long u64;
typedef unsigned short u16;
typedef __bf16 bf2 __attribute__((ext_vector_type(2)));
typedef __bf16 bf8 __attribute__((ext_vector_type(8)));
typedef short s4 __attribute__((ext_vector_type(4)));
typedef short s8 __attribute__((ext_vector_type(8)));
typedef float f2 __attribute__((ext_vector_type(2)));
typedef float f4 __attribute__((ext_vector_type(4)));
typedef float f16v __attribute__((ext_vector_type(16)));
typedef u32 u32x2 __attribute__((ext_vector_type(2)));
typedef u32 u32x4 __attribute__((ext_vector_type(4)));
#define LAS __attribute__((address_space(3)))
#define DI __device__ __forceinline__

constexpr u32 WS_EPOCH = 0;
constexpr u32 WS_FK = 0x1000;
constexpr u32 WS_FQ = 0x2000;
constexpr u32 WS_FF = 0x4000;
constexpr u32 WS_FO = 0x5000;
constexpr u32 WS_OMG = 0x6000;
constexpr u32 WS_FH = 0x8000;
constexpr u32 WS_KVF = 0x20000;
constexpr u32 WS_KVP = 0x100000;
constexpr u32 WS_PQ = 0x300000;
constexpr u32 WS_HB = 0x800000;
constexpr u32 WS_OB = 0x1000000;
constexpr u32 WS_DBG = 0x1200000;
constexpr u32 WS_BYTES = 0x1300000;

constexpr u32 L_R0 = 0, L_R1 = 57344;
constexpr u32 L_Z0 = 16384, L_Z1 = 49152, L_Z2 = 81920, L_Z3 = 114688;
constexpr u32 L_WQ = 114688, L_PHK = 143360, L_MISC = 159744, L_V = L_R1;
constexpr u32 L_WPHI = L_MISC, L_GW = L_MISC + 2048, L_B1 = L_MISC + 3072, L_B2 = L_MISC + 3328, L_G = L_MISC + 3584;
constexpr u32 L_RED0 = 0, L_RED1 = L_WQ + 1024;
constexpr u32 LDS_BYTES = 163840;

constexpr u32 SPIN_CAP = 1u << 15;
constexpr float LOG2E = 1.4426950408889634f;

#define MFMA(a, b, c) __builtin_amdgcn_mfma_f32_32x32x16_bf16(__builtin_bit_cast(bf8, a), __builtin_bit_cast(bf8, b), c, 0, 0, 0)
#define SBAR() __builtin_amdgcn_sched_barrier(0)
#define WAITV(N) asm volatile("s_waitcnt vmcnt(%0)" ::"i"(N) : "memory")
#define WAITV_BAR(N) asm volatile("s_waitcnt vmcnt(%0) lgkmcnt(0)\n\ts_barrier" ::"i"(N) : "memory")
#define BAR() asm volatile("s_waitcnt lgkmcnt(0)\n\ts_barrier" ::: "memory")

#define STAMP(k) do { u64 t_; asm volatile("s_memrealtime %0\n\ts_waitcnt lgkmcnt(0)" : "=s"(t_) :: "memory"); st[k] = (u32)t_; } while (0)

DI u32x4 mksrd(const void* p, u32 bytes) {
  u64 a = (u64)p;
  return (u32x4){(u32)a, (u32)(a >> 32) & 0xffffu, bytes, 0x00020000u};
}
DI void dma(u32 m0v, u32 voff, u32x4 srd, u32 soff) {
  asm volatile("s_mov_b32 m0, %0\n\ts_nop 0\n\tbuffer_load_dwordx4 %1, %2, %3 offen lds" ::"s"(m0v), "v"(voff), "s"(srd), "s"(soff) : "memory");
}
template <int OFF> DI void ld16(u32x4& d, u32 voff, u32x4 srd, u32 soff) {
  asm volatile("buffer_load_dwordx4 %0, %1, %2, %3 offen offset:%4 sc1" : "=v"(d) : "v"(voff), "s"(srd), "s"(soff), "i"(OFF) : "memory");
}
DI void ld8(u32x2& d, u32 voff, u32x4 srd, u32 soff) {
  asm volatile("buffer_load_dwordx2 %0, %1, %2, %3 offen sc1" : "=v"(d) : "v"(voff), "s"(srd), "s"(soff) : "memory");
}
DI void ld4(u32& d, u32 voff, u32x4 srd, u32 soff) {
  asm volatile("buffer_load_dword %0, %1, %2, %3 offen sc1" : "=v"(d) : "v"(voff), "s"(srd), "s"(soff) : "memory");
}
template <int OFF> DI void st16(u32x4 d, u32 voff, u32x4 srd, u32 soff) {
  asm volatile("buffer_store_dwordx4 %0, %1, %2, %3 offen offset:%4 sc1\n\ts_nop 1" ::"v"(d), "v"(voff), "s"(srd), "s"(soff), "i"(OFF) : "memory");
}
DI void st8(u32x2 d, u32 voff, u32x4 srd, u32 soff) {
  asm volatile("buffer_store_dwordx2 %0, %1, %2, %3 offen sc1\n\ts_nop 1" ::"v"(d), "v"(voff), "s"(srd), "s"(soff) : "memory");
}
DI void st4(u32 d, u32 voff, u32x4 srd, u32 soff) {
  asm volatile("buffer_store_dword %0, %1, %2, %3 offen sc1\n\ts_nop 1" ::"v"(d), "v"(voff), "s"(srd), "s"(soff) : "memory");
}
template <int N> DI void waitr(u32x4& a) { asm volatile("s_waitcnt vmcnt(%1)" : "+v"(a) : "i"(N) : "memory"); }
template <int N> DI void waitr(u32x2& a) { asm volatile("s_waitcnt vmcnt(%1)" : "+v"(a) : "i"(N) : "memory"); }
DI void pin(u32x4& a) { asm volatile("" : "+v"(a)); }

DI void flag(u32x2 tagv, u32 flane, u32x4 srd, u32 soff) { st8(tagv, flane, srd, soff); }
DI u64 tagmask(u32x2 p, u32x2 tagv) { return __ballot(p.x == tagv.x && p.y == tagv.y); }
DI u64 spin(u64 m, u64 need, u32 voff, u32x4 srd, u32 soff, u32x2 tagv) {
  for (u32 it = 0; (m & need) != need && it < SPIN_CAP; ++it) {
    u32x2 p;
    ld8(p, voff, srd, soff);
    waitr<0>(p);
    m = tagmask(p, tagv);
  }
  return m;
}

DI u32 pk(float a, float b) { return __builtin_bit_cast(u32, __builtin_convertvector((f2){a, b}, bf2)); }
DI u32x4 pk8(const f16v& v, int o) { return (u32x4){pk(v[o], v[o + 1]), pk(v[o + 2], v[o + 3]), pk(v[o + 4], v[o + 5]), pk(v[o + 6], v[o + 7])}; }
DI u32x2 trd(LAS char* p) { return __builtin_bit_cast(u32x2, __builtin_amdgcn_ds_read_tr16_b64_v4i16((LAS s4*)p)); }
DI u32x4 tr2(LAS char* p, LAS char* q) { u32x2 a = trd(p), b = trd(q); return (u32x4){a.x, a.y, b.x, b.y}; }
DI float blo(u32 v) { return __uint_as_float(v << 16); }
DI float bhi(u32 v) { return __uint_as_float(v & 0xffff0000u); }
DI float xsum(float v) { auto r = __builtin_amdgcn_permlane32_swap(__float_as_uint(v), __float_as_uint(v), false, false); return __uint_as_float(r[0]) + __uint_as_float(r[1]); }
DI float xhi(float v) { auto r = __builtin_amdgcn_permlane32_swap(__float_as_uint(v), __float_as_uint(v), false, false); return __uint_as_float(r[1]); }
DI float mx3(float a, float b, float c) { return __builtin_fmaxf(__builtin_fmaxf(a, b), c); }
DI float mn3(float a, float b, float c) { return __builtin_fminf(__builtin_fminf(a, b), c); }
DI float dot2(u32 a, u32 b, float c) { return __builtin_amdgcn_fdot2_f32_bf16(__builtin_bit_cast(bf2, a), __builtin_bit_cast(bf2, b), c, false); }

DI f16v bias16(LAS char* p) {
  f16v a;
#pragma unroll
  for (int g = 0; g < 4; ++g) {
    u32x2 v = *(LAS u32x2*)(p + 16 * g);
    a[4 * g] = blo(v.x); a[4 * g + 1] = bhi(v.x); a[4 * g + 2] = blo(v.y); a[4 * g + 3] = bhi(v.y);
  }
  return a;
}
DI float gelu1(float x, float cg) {
  const float C0 = -2.0f * 0.7978845608028654f * LOG2E, C1 = C0 * 0.044715f;
  float t = __builtin_fmaf(x * x, C1, C0);
  float e = __builtin_amdgcn_exp2f(x * t);
  return x * __builtin_amdgcn_rcpf(__builtin_fmaf(e, cg, cg));
}
DI void featmap(const f16v& a, LAS char* wp, bool hi, f16v& ep, f16v& en) {
  u32x4 w0 = tr2(wp, wp + 8 * 56);
  u32x4 w1 = tr2(wp + 16 * 56, wp + 24 * 56);
  w1.z = hi ? 0u : w1.z; w1.w = hi ? 0u : w1.w;
  f16v p = {};
  p = MFMA(w0, pk8(a, 0), p);
  p = MFMA(w1, pk8(a, 8), p);
  float mx = mx3(p[0], p[1], p[2]), mn = mn3(p[0], p[1], p[2]);
#pragma unroll
  for (int i = 3; i < 15; i += 2) { mx = mx3(mx, p[i], p[i + 1]); mn = mn3(mn, p[i], p[i + 1]); }
  mx = __builtin_fmaxf(mx, p[15]); mn = __builtin_fminf(mn, p[15]);
  { auto r = __builtin_amdgcn_permlane32_swap(__float_as_uint(mx), __float_as_uint(mx), false, false); mx = __builtin_fmaxf(__uint_as_float(r[0]), __uint_as_float(r[1])); }
  { auto r = __builtin_amdgcn_permlane32_swap(__float_as_uint(mn), __float_as_uint(mn), false, false); mn = __builtin_fminf(__uint_as_float(r[0]), __uint_as_float(r[1])); }
  const float a0 = -mx * LOG2E, a1 = mn * LOG2E;
#pragma unroll
  for (int i = 0; i < 16; ++i) {
    ep[i] = __builtin_amdgcn_exp2f(__builtin_fmaf(p[i], LOG2E, a0));
    en[i] = __builtin_amdgcn_exp2f(__builtin_fmaf(p[i], -LOG2E, a1));
  }
}
DI float sum14(const f16v& e, bool hi) {
  float a = (e[0] + e[1]) + (e[2] + e[3]), b = (e[4] + e[5]) + (e[6] + e[7]), c = (e[8] + e[9]) + (e[10] + e[11]), d = (e[12] + e[13]) + (e[14] + e[15]);
  return xsum((a + b) + (c + (hi ? 0.f : d)));
}

__global__ __launch_bounds__(512, 2) void fused_kernel(const u16* __restrict__ x, const u16* __restrict__ wqkv, const u16* __restrict__ wphi,
                                                       const u16* __restrict__ wo, const u16* __restrict__ w1, const u16* __restrict__ b1,
                                                       const u16* __restrict__ w2, const u16* __restrict__ b2, const u16* __restrict__ gw,
                                                       u16* __restrict__ out, char* __restrict__ ws) {
  __shared__ __attribute__((aligned(16))) char smem_[LDS_BYTES];
  LAS char* sm = (LAS char*)smem_;
  const u32 lds0 = (u32)(size_t)sm;
  u32 st[16];
#pragma unroll
  for (int k = 0; k < 16; ++k) st[k] = 0;
  STAMP(0);
  u64 c0_ = __builtin_amdgcn_s_memtime();
  const u32 tid = threadIdx.x, lane = tid & 63;
  const u32 w = __builtin_amdgcn_readfirstlane(tid >> 6), ch = w >> 2, tg = w & 3;
  const u32 b = blockIdx.x, xcd = b & 7, rr = b >> 3;
  const u32 t = (xcd & 1) * 8 + (rr & 7), hd = (xcd >> 1) * 4 + (rr >> 3);
  const bool special = ((hd & 3) == 3) && (((hd >> 2) & 1) == (t & 1));
  const u32 js = special ? 0u : ((t & 1) ? hd - (hd > 7) : hd - (hd > 3) - (hd > 11));
  const u32 nu = rr < 24 ? rr : 24 + ((rr & 7) >> 1);
  const u32 ut = xcd * 4 + (nu & 3), ct = nu >> 2;
  const u32 r = lane & 31, h = lane >> 5, grp = (lane >> 4) & 1, q = (lane >> 2) & 3, p = lane & 3;
  const bool hi = h != 0;

  const u32x4 sX = mksrd(x, 2048 * 896), sQKV = mksrd(wqkv, 448 * 2688), sW1 = mksrd(w1, special ? 0u : 448u * 3584u), sWS = mksrd(ws, WS_BYTES);

  u32x2 tagv;
  ld8(tagv, 0u, sWS, WS_EPOCH);
  if (w < 4) {
    const char* g;
    if (w == 0) g = (const char*)wphi + hd * 1568 + lane * 16;
    else if (w == 1) g = (const char*)wphi + hd * 1568 + 1024 + (lane < 33 ? lane : 33) * 16;
    else if (w == 2) g = (const char*)gw + (lane < 55 ? lane : 55) * 16;
    else g = (lane >= 16 && lane < 24) ? (const char*)b2 + ct * 128 + (lane - 16) * 16 : (const char*)b1 + js * 256 + (lane & 15) * 16;
    if (w < 3 || lane < 32)
      asm volatile("s_mov_b32 m0, %0\n\ts_nop 0\n\tglobal_load_lds_dwordx4 %1, off" ::"s"(lds0 + L_MISC + w * 1024), "v"(g) : "memory");
  }
  const u32 l8 = lane >> 3, c8 = lane & 7;
  const u32 vx0 = (t * 128 + 16 * w + l8) * 896 + ((c8 ^ (lane >> 4)) << 4);
  const u32 vx1 = (t * 128 + 16 * w + 8 + l8) * 896 + ((c8 ^ (4 + (lane >> 4))) << 4);
  const u32 c4 = p == 3 ? 40u : p * 16u;
  const u32 vq = (lane >> 2) * 2688 + hd * 56 + c4;
  const u32 vkv = vq + 16 * tg * 2688 + 896 * (1 + ch);
  const u32 sw8 = (c8 ^ ((l8 & 2) << 1)) << 4;
  const u32 vw1 = (8 * w + l8) * 3584 + js * 256 + sw8;
  const u32 mw = lds0 + w * 1024;

#define CHUNK(c, Z)                                           \
  do {                                                        \
    dma(lds0 + (Z) + w * 2048, vx0, sX, (c) * 128);           \
    dma(lds0 + (Z) + w * 2048 + 1024, vx1, sX, (c) * 128);    \
    dma(mw + (Z) + 16384, vkv, sQKV, (c) * 64 * 2688);        \
    dma(mw + (Z) + 24576, vw1, sW1, (c) * 64 * 3584);         \
  } while (0)
#define L1U(u) dma(mw + L_R0 + (u) * 8192, vw1, sW1, (u) * 64 * 3584 + 128)
  CHUNK(0, L_Z0); CHUNK(1, L_Z1); CHUNK(2, L_Z2); CHUNK(3, L_Z3);

  const u32 ax = (tg * 32 + r) * 128 + ((h ^ ((r >> 1) & 7)) << 4);
  const u32 sl = 4 * grp + p;
  const u32 a64 = (8 * h + q) * 64 + ((sl ^ (sl >= 6 ? 1u : 0u)) << 3);
  const u32 a128 = (8 * h + q) * 128 + ((ch ^ (q >> 1)) << 6) + grp * 32 + p * 8;
  const u32 flane = lane == 0 ? 0u : 0x80000000u;

  u32x4 xf[28];
  f16v accA = {}, accH, accH1;
  float gl = 0.f;
#define STEP(c, Z)                                                                                   \
  do {                                                                                               \
    _Pragma("unroll") for (int s = 0; s < 4; ++s) {                                                  \
      xf[4 * (c) + s] = *(LAS u32x4*)(sm + (Z) + (ax ^ (32 * s)));                                   \
      u32x4 gf = *(LAS u32x4*)(sm + L_GW + (c) * 128 + s * 32 + h * 16);                             \
      LAS char* pa = sm + (Z) + 16384 + ch * 4096 + s * 1024 + a64;                                  \
      LAS char* ph = sm + (Z) + 24576 + s * 2048 + a128;                                             \
      u32x4 wa = tr2(pa, pa + 256), wh = tr2(ph, ph + 512);                                          \
      accA = MFMA(wa, xf[4 * (c) + s], accA);                                                        \
      accH = MFMA(wh, xf[4 * (c) + s], accH);                                                        \
      _Pragma("unroll") for (int e = 0; e < 4; ++e) gl = dot2(xf[4 * (c) + s][e], gf[e], gl);        \
    }                                                                                                \
  } while (0)

  waitr<12>(tagv);
  BAR();
  STAMP(1);
  {
    u64 e = ((u64)__builtin_amdgcn_readfirstlane(tagv.y) << 32 | __builtin_amdgcn_readfirstlane(tagv.x)) + 0x9E3779B97F4A7C15ull;
    tagv = (u32x2){(u32)e, (u32)(e >> 32)};
  }
  accH = bias16(sm + L_B1 + ch * 64 + h * 8);
  STEP(0, L_Z0);
  WAITV_BAR(8); CHUNK(4, L_Z0); STEP(1, L_Z1);
  WAITV_BAR(8); CHUNK(5, L_Z1); STEP(2, L_Z2);
  WAITV_BAR(8); CHUNK(6, L_Z2); STEP(3, L_Z3);
  WAITV_BAR(8);
#pragma unroll
  for (int n = 0; n < 3; ++n) dma(mw + L_WQ + n * 8192, vq, sQKV, (n * 8 + w) * 16 * 2688);
  if (ch == 0) dma(mw + L_WQ + 3 * 8192, vq, sQKV, (24 + w) * 16 * 2688);
  STEP(4, L_Z0);
  WAITV_BAR(7); L1U(0); L1U(1); L1U(2); L1U(3); STEP(5, L_Z1);
  WAITV_BAR(7); L1U(4); L1U(5); L1U(6); STEP(6, L_Z2);
  STAMP(2);
  accH1 = bias16(sm + L_B1 + 128 + ch * 64 + h * 8);
  gl = xsum(gl);
  const float cg = __builtin_fminf(1.f + __builtin_amdgcn_exp2f(gl * LOG2E), 3.0e38f);

  const u32x4 sW2 = mksrd(w2, 1792 * 896);
  const u32 vb = l8 * 896 + ct * 128 + sw8;
  u32 rowoff[7];
#pragma unroll
  for (int u = 0; u < 7; ++u) { u32 m = 7 * tg + u; rowoff[u] = (128 * (m >> 1) + 16 * (m & 1)) * 896; }
  const u32 mb = lds0 + tg * 2048;
  const u32 l16 = lane * 16;
  const u32 atx = (tg * 32 + r) * 64 + ((h ^ ((r >> 1) & 7)) << 3);
  LAS char* wp = sm + L_WPHI + (4 * h + q) * 56 + (sl == 7 ? 48u : sl * 8);
  const u32 hbP = WS_HB + ((t * 4 + tg) * 4 * 28 + 2 * js) * 1024;
  const u32 fhP = WS_FH + (t * 4 + tg) * 512 + js * 8;

  if (ch == 0) {
    f16v ep, en;
    featmap(accA, wp, hi, ep, en);
    const float rp = __builtin_amdgcn_rcpf(sum14(ep, hi)), rn = __builtin_amdgcn_rcpf(sum14(en, hi));
#pragma unroll
    for (int g = 0; g < 4; ++g) {
      *(LAS u32x2*)(sm + L_PHK + (atx ^ (16 * g))) = (u32x2){pk(ep[4 * g] * rp, ep[4 * g + 1] * rp), pk(ep[4 * g + 2] * rp, ep[4 * g + 3] * rp)};
      *(LAS u32x2*)(sm + L_PHK + 8192 + (atx ^ (16 * g))) = (u32x2){pk(en[4 * g] * rn, en[4 * g + 1] * rn), pk(en[4 * g + 2] * rn, en[4 * g + 3] * rn)};
    }
    if (!hi) *(LAS float*)(sm + L_G + (tg * 32 + r) * 4) = gl;
    WAITV_BAR(7);
    STAMP(3);
    if (tg < 2) {
      const u32 a0 = (8 * h + q) * 64 + ((sl ^ (4 * h + (q >> 1))) << 3), a1 = (a0 ^ 16) + 256;
      f16v kv = {};
#pragma unroll
      for (int ks = 0; ks < 8; ++ks) {
        u32x4 fa = tr2(sm + L_PHK + tg * 8192 + ks * 1024 + a0, sm + L_PHK + tg * 8192 + ks * 1024 + a1);
        u32x4 fb = tr2(sm + L_V + ks * 1024 + a0, sm + L_V + ks * 1024 + a1);
        kv = MFMA(fa, fb, kv);
      }
      u32x4 o0 = __builtin_bit_cast(u32x4, (f4){kv[0], kv[1], kv[2], kv[3]}), o1 = __builtin_bit_cast(u32x4, (f4){kv[4], kv[5], kv[6], kv[7]});
      u32x4 o2 = __builtin_bit_cast(u32x4, (f4){kv[8], kv[9], kv[10], kv[11]}), o3 = __builtin_bit_cast(u32x4, (f4){kv[12], kv[13], kv[14], kv[15]});
      asm volatile("s_nop 7\n\ts_nop 4" : "+v"(o0), "+v"(o1), "+v"(o2), "+v"(o3));
      const u32 so = WS_KVP + ((hd * 2 + tg) * 16 + t) * 4096;
      st16<0>(o0, l16, sWS, so); st16<1024>(o1, l16, sWS, so); st16<2048>(o2, l16, sWS, so); st16<3072>(o3, l16, sWS, so);
    }
  } else {
    accA[12] = hi ? 1.0f : accA[12];
#pragma unroll
    for (int g = 0; g < 4; ++g)
      *(LAS u32x2*)(sm + L_V + (atx ^ (16 * g))) = (u32x2){pk(accA[4 * g], accA[4 * g + 1]), pk(accA[4 * g + 2], accA[4 * g + 3])};
  }
  if (!special) {
    const u32 vb0 = vb + 32 * 896, vb1 = vb0 + 8 * 896;
#pragma unroll
    for (int g = 0; g < 4; ++g) {
      if (ch == 0) {
#pragma unroll
        for (int n = 3 * g; n < 3 * g + 3; ++n) dma(mb + L_R1 + (1 + (n >> 1)) * 8192 + (n & 1) * 1024, (n & 1) ? vb1 : vb0, sW2, rowoff[1 + (n >> 1)]);
      }
#pragma unroll
      for (int e = 0; e < 4; ++e) accH[4 * g + e] = gelu1(accH[4 * g + e], cg);
      SBAR();
    }
    const u32 so = hbP + (1 - ch) * 28 * 1024;
    st16<0>(pk8(accH, 0), l16, sWS, so);
    st16<1024>(pk8(accH, 8), l16, sWS, so);
  }
  if (ch == 0) {
    if (special) WAITV(0); else WAITV(14);
    if (tg < 2) flag(tagv, flane, sWS, WS_FK + ((hd * 2 + tg) * 16 + t) * 8);
    if (!special) BAR();
    STAMP(4);
  } else {
    if (special) WAITV_BAR(7); else WAITV_BAR(9);
    STAMP(3);
    f16v aq = {};
#pragma unroll
    for (int s = 0; s < 28; ++s) {
      LAS char* pa = sm + L_WQ + s * 1024 + a64;
      aq = MFMA(tr2(pa, pa + 256), xf[s], aq);
    }
    if (!special) {
      WAITV(0);
      flag(tagv, flane, sWS, fhP);
      BAR();
      STAMP(4);
      dma(mb + L_R1, vb + 32 * 896, sW2, rowoff[0]);
      dma(mb + L_R1 + 1024, vb + 40 * 896, sW2, rowoff[0]);
    }
    f16v ep, en;
    featmap(aq, wp, hi, ep, en);
    const u32 so = WS_PQ + ((t * 16 + hd) * 4 + tg) * 4096;
    st16<0>(pk8(ep, 0), l16, sWS, so); st16<1024>(pk8(ep, 8), l16, sWS, so);
    st16<2048>(pk8(en, 0), l16, sWS, so); st16<3072>(pk8(en, 8), l16, sWS, so);
    if (!special) BAR();
    else { WAITV(0); flag(tagv, flane, sWS, WS_FQ + ((t * 16 + hd) * 4 + tg) * 8); }
  }

  if (special) {
    const u32 half = hd >> 3;
    if (ch == 0) {
      spin(0, 0xffffull, (lane & 15) * 8, sWS, WS_FK + (t * 2 + half) * 128, tagv);
      STAMP(5);
      u32x4 v[16];
      const u32 so = WS_KVP + (t * 2 + half) * 65536 + tg * 1024;
#pragma unroll
      for (int k = 0; k < 16; ++k) ld16<0>(v[k], l16, sWS, so + k * 4096);
      waitr<0>(v[0]);
#pragma unroll
      for (int k = 1; k < 16; ++k) pin(v[k]);
      f4 a = __builtin_bit_cast(f4, v[0]);
#pragma unroll
      for (int k = 1; k < 16; ++k) a += __builtin_bit_cast(f4, v[k]);
      u32x2 o = {pk(a[0], a[1]), pk(a[2], a[3])};
      if (r == 29) o = (u32x2){0x3f803f80u, 0x3f803f80u};
      if (hi && tg == 3) o = (u32x2){0u, 0u};
      st8(o, l16, sWS, WS_KVF + (t * 2 + half) * 2048 + (tg >> 1) * 1024 + (tg & 1) * 8);
      WAITV(0);
      flag(tagv, flane, sWS, WS_FF + t * 128 + (half * 4 + tg) * 8);
      STAMP(6);
    }
    const u32 oh = half * 8 + w;
    spin(0, 0xfull, (lane & 3) * 8, sWS, WS_FQ + (t * 16 + oh) * 32, tagv);
    u32x4 e[16];
    {
      const u32 so = WS_PQ + (t * 16 + oh) * 16384;
#pragma unroll
      for (int k = 0; k < 4; ++k) {
        ld16<0>(e[4 * k], l16, sWS, so + k * 4096); ld16<1024>(e[4 * k + 1], l16, sWS, so + k * 4096);
        ld16<2048>(e[4 * k + 2], l16, sWS, so + k * 4096); ld16<3072>(e[4 * k + 3], l16, sWS, so + k * 4096);
      }
    }
    WAITV(0);
#pragma unroll
    for (int k = 0; k < 16; ++k) pin(e[k]);
    STAMP(7);
    spin(0, 0xffull, (lane & 7) * 8, sWS, WS_FF + oh * 128, tagv);
    STAMP(8);
    u32x4 kf[4];
    ld16<0>(kf[0], l16, sWS, WS_KVF + oh * 4096); ld16<1024>(kf[1], l16, sWS, WS_KVF + oh * 4096);
    ld16<2048>(kf[2], l16, sWS, WS_KVF + oh * 4096); ld16<3072>(kf[3], l16, sWS, WS_KVF + oh * 4096);
    if (half == 0 && ch == 0 && !hi) st4(__float_as_uint(__builtin_amdgcn_rcpf(cg)), r * 4, sWS, WS_OMG + (t * 128 + tg * 32) * 4);
    WAITV(0);
    pin(kf[0]); pin(kf[1]); pin(kf[2]); pin(kf[3]);
#pragma unroll
    for (int k = 0; k < 4; ++k) {
      f16v ap = {}, an = {};
      ap = MFMA(kf[0], e[4 * k], ap); an = MFMA(kf[2], e[4 * k + 2], an);
      ap = MFMA(kf[1], e[4 * k + 1], ap); an = MFMA(kf[3], e[4 * k + 3], an);
      const float lg = *(LAS float*)(sm + L_G + (k * 32 + r) * 4);
      const float g = __builtin_amdgcn_rcpf(1.f + __builtin_amdgcn_exp2f(-lg * LOG2E));
      const float ip = __builtin_amdgcn_rcpf(ap[13]), in = __builtin_amdgcn_rcpf(an[13]);
      const float sc = g * __builtin_amdgcn_rcpf(ap[12] * ip + an[12] * in + 1e-6f);
      const float al = xhi(sc * ip), be = xhi(sc * in);
      f16v o;
#pragma unroll
      for (int i = 0; i < 16; ++i) o[i] = ap[i] * al + an[i] * be;
      u32x4 o0 = pk8(o, 0), o1 = pk8(o, 8);
      o1.z = hi ? 0u : o1.z; o1.w = hi ? 0u : o1.w;
      const u32 so = WS_OB + ((t * 4 + k) * 16 + oh) * 2048;
      st16<0>(o0, l16, sWS, so); st16<1024>(o1, l16, sWS, so);
    }
    WAITV(0);
    flag(tagv, flane, sWS, WS_FO + (t * 16 + oh) * 8);
    STAMP(9);
  } else {
    const u32 tgG = ut * 2 + ch;
    const u32 fhC = WS_FH + tgG * 512;
    u32x2 pl;
#pragma unroll
    for (int s = 0; s < 28; ++s) {
      LAS char* ph = sm + L_R0 + s * 2048 + a128;
      accH1 = MFMA(tr2(ph, ph + 512), xf[s], accH1);
      if (s == 18) { SBAR(); ld8(pl, lane * 8, sWS, fhC); SBAR(); }
    }
    waitr<0>(pl);
    flag(tagv, flane, sWS, ch == 0 ? fhP + 128 : WS_FQ + ((t * 16 + hd) * 4 + tg) * 8);
    BAR();
    STAMP(5);
    u64 fm = spin(tagmask(pl, tagv), 0x3fffull, lane * 8, sWS, fhC, tagv);
    u32x4 A0[7], A1[7], A2[7], A3[7], A4[8];
    const u32 hbC = WS_HB + (tgG * 4 * 28 + 7 * tg) * 1024;
#define LOADA(A, so)                                                                                                    \
  do {                                                                                                                  \
    ld16<0>(A[0], l16, sWS, (so)); ld16<1024>(A[1], l16, sWS, (so)); ld16<2048>(A[2], l16, sWS, (so));                  \
    ld16<3072>(A[3], l16, sWS, (so)); ld16<0>(A[4], l16, sWS, (so) + 4096); ld16<1024>(A[5], l16, sWS, (so) + 4096);    \
    ld16<2048>(A[6], l16, sWS, (so) + 4096);                                                                            \
  } while (0)
    LOADA(A0, hbC);
    STAMP(6);
#pragma unroll
    for (int e = 0; e < 16; ++e) accH1[e] = gelu1(accH1[e], cg);
    {
      const u32 so = hbP + (2 + ch) * 28 * 1024;
      st16<0>(pk8(accH1, 0), l16, sWS, so);
      st16<1024>(pk8(accH1, 8), l16, sWS, so);
    }
    if (ch == 0) {
      BAR();
      ld8(pl, lane * 8, sWS, fhC);
#pragma unroll
      for (int n = 0; n < 14; ++n) dma(mb + L_R0 + (n >> 1) * 8192 + (n & 1) * 1024, (n & 1) ? vb + 8 * 896 : vb, sW2, rowoff[n >> 1]);
      waitr<14>(pl);
      flag(tagv, flane, sWS, fhP + 256);
      fm = tagmask(pl, tagv);
    }
    fm = spin(fm, 0x3fffull << 16, lane * 8, sWS, fhC, tagv);
    LOADA(A1, hbC + 28 * 1024);
    STAMP(7);
    f16v acc0 = {}, acc1 = {};
    const u32 ab0 = tg * 2048 + (4 * h + q) * 128 + ((q >> 1) << 6) + grp * 32 + p * 8, ab1 = ab0 ^ 64;
#define SEG(A, R, N0)                                                   \
  do {                                                                  \
    _Pragma("unroll") for (int u = 0; u < 7; ++u) {                     \
      LAS char* p0 = sm + (R) + u * 8192 + ab0;                         \
      LAS char* p1 = sm + (R) + u * 8192 + ab1;                         \
      u32x4 f0 = tr2(p0, p0 + 1024), f1 = tr2(p1, p1 + 1024);           \
      asm volatile("s_waitcnt vmcnt(%1)" : "+v"(A[u]) : "i"((N0) + 6 - u)); \
      acc0 = MFMA(f0, A[u], acc0);                                      \
      acc1 = MFMA(f1, A[u], acc1);                                      \
    }                                                                   \
  } while (0)
    const u32 vbh = vb + ch * 8 * 896, mbh = mb + ch * 1024;
#define STAGEB(R, so) _Pragma("unroll") for (int u = 0; u < 7; ++u) dma(mbh + (R) + u * 8192, vbh + (so) * 896, sW2, rowoff[u])
    SEG(A0, L_R1, 9);
    WAITV(7);
    if (ch == 1) flag(tagv, flane, sWS, fhP + 384);
    BAR();
    STAMP(8);
    ld8(pl, lane * 8, sWS, fhC);
    STAGEB(L_R1, 64);
    waitr<7>(pl);
    fm = spin(tagmask(pl, tagv), 0x3fffull << 32, lane * 8, sWS, fhC, tagv);
    LOADA(A2, hbC + 2 * 28 * 1024);
    SEG(A1, L_R0, 14);
    WAITV_BAR(7);
    STAMP(9);
    ld8(pl, lane * 8, sWS, fhC);
    STAGEB(L_R0, 96);
    SEG(A2, L_R1, 8);
    waitr<7>(pl);
    fm = spin(tagmask(pl, tagv), 0x3fffull << 48, lane * 8, sWS, fhC, tagv);
    LOADA(A3, hbC + 3 * 28 * 1024);
    WAITV_BAR(7);
    STAMP(10);
    const u32 foC = WS_FO + (ut >> 1) * 128;
    ld8(pl, (lane & 15) * 8, sWS, foC);
    {
      const u32x4 sWO = mksrd(wo, 448 * 896);
#pragma unroll
      for (int u = 0; u < 7; ++u) dma(lds0 + L_R1 + tg * 14336 + ch * 1024 + u * 2048, vbh, sWO, (tg * 112 + u * 16) * 896);
    }
    SEG(A3, L_R0, 8);
    waitr<7>(pl);
    spin(tagmask(pl, tagv), 0xffffull, (lane & 15) * 8, sWS, foC, tagv);
    STAMP(11);
    u32 omg;
    ld4(omg, r * 4, sWS, WS_OMG + tgG * 128);
    {
      const u32 so = WS_OB + (tgG * 16 + 4 * tg) * 2048;
      LOADA(A4, so);
      ld16<3072>(A4[7], l16, sWS, so + 4096);
    }
    WAITV_BAR(9);
    STAMP(12);
    {
      const u32 ao0 = ab0 + tg * (14336 - 2048), ao1 = ao0 ^ 64;
#pragma unroll
      for (int u = 0; u < 8; ++u) {
        LAS char* p0 = sm + L_R1 + (28 * (u >> 1) + 16 * (u & 1)) * 128 + ao0;
        LAS char* p1 = sm + L_R1 + (28 * (u >> 1) + 16 * (u & 1)) * 128 + ao1;
        u32x4 f0 = tr2(p0, p0 + 1024), f1 = tr2(p1, p1 + 1024);
        asm volatile("s_waitcnt vmcnt(%1)" : "+v"(A4[u]) : "i"(7 - u));
        acc0 = MFMA(f0, A4[u], acc0);
        acc1 = MFMA(f1, A4[u], acc1);
      }
    }
    asm volatile("" : "+v"(omg));
    STAMP(13);
    LAS char* red = sm + (ch ? L_RED1 : L_RED0) + l16;
#pragma unroll
    for (int g = 0; g < 4; ++g) {
      *(LAS f4*)(red + (g >> 1) * 8192 + tg * 2048 + (g & 1) * 1024) = (f4){acc0[4 * g], acc0[4 * g + 1], acc0[4 * g + 2], acc0[4 * g + 3]};
      *(LAS f4*)(red + (2 + (g >> 1)) * 8192 + tg * 2048 + (g & 1) * 1024) = (f4){acc1[4 * g], acc1[4 * g + 1], acc1[4 * g + 2], acc1[4 * g + 3]};
    }
    const u32x2 bA = *(LAS u32x2*)(sm + L_B2 + tg * 32 + h * 8), bB = *(LAS u32x2*)(sm + L_B2 + tg * 32 + 16 + h * 8);
    BAR();
    const float om = __uint_as_float(omg);
    f4 sa = (f4){blo(bA.x), bhi(bA.x), blo(bA.y), bhi(bA.y)} * om, sb = (f4){blo(bB.x), bhi(bB.x), blo(bB.y), bhi(bB.y)} * om;
#pragma unroll
    for (int k = 0; k < 4; ++k) {
      sa += *(LAS f4*)(red + tg * 8192 + k * 2048);
      sb += *(LAS f4*)(red + tg * 8192 + k * 2048 + 1024);
    }
    auto s0 = __builtin_amdgcn_permlane32_swap(pk(sa[0], sa[1]), pk(sb[0], sb[1]), false, false);
    auto s1 = __builtin_amdgcn_permlane32_swap(pk(sa[2], sa[3]), pk(sb[2], sb[3]), false, false);
    *(u32x4*)((char*)out + (tgG * 32 + r) * 896 + ct * 128 + tg * 32 + h * 16) = (u32x4){s0[0], s1[0], s0[1], s1[1]};
    if (b == 0 && w == 0) flag(tagv, flane, sWS, WS_EPOCH);
    STAMP(14);
  }
  WAITV(0);
  STAMP(15);
  if (b == 0 && w == 0) { u32 dt = 0u * (st[15] - st[0]) + (st[1]==12345) + (st[2]==12345)+ (st[8]==12345); while ((u32)__builtin_amdgcn_s_memrealtime() - st[15] < dt) __builtin_amdgcn_s_sleep(1); }
}

extern "C" void kernel_launch(void* const* d_in, const int* in_sizes, int n_in, void* d_out, int out_size, void* d_ws, size_t ws_size,
                              hipStream_t stream) {
  fused_kernel<<<256, 512, 0, stream>>>((const u16*)d_in[0], (const u16*)d_in[1], (const u16*)d_in[2], (const u16*)d_in[3], (const u16*)d_in[5],
                                        (const u16*)d_in[6], (const u16*)d_in[7], (const u16*)d_in[8], (const u16*)d_in[9], (u16*)d_out,
                                        (char*)d_ws);
}
```

```cpp
#include <hip/hip_runtime.h>
#include <stdint.h>

typedef unsigned u32;
typedef unsigned long long u64;
typedef unsigned short u16;
typedef __bf16 bf2 __attribute__((ext_vector_type(2)));
typedef __bf16 bf8 __attribute__((ext_vector_type(8)));
typedef short s4 __attribute__((ext_vector_type(4)));
typedef short s8 __attribute__((ext_vector_type(8)));
typedef float f2 __attribute__((ext_vector_type(2)));
typedef float f4 __attribute__((ext_vector_type(4)));
typedef float f16v __attribute__((ext_vector_type(16)));
typedef u32 u32x2 __attribute__((ext_vector_type(2)));
typedef u32 u32x4 __attribute__((ext_vector_type(4)));
#define LAS __attribute__((address_space(3)))
#define DI __device__ __forceinline__

constexpr u32 WS_EPOCH = 0;
constexpr u32 WS_FK = 0x1000;
constexpr u32 WS_FQ = 0x2000;
constexpr u32 WS_FF = 0x4000;
constexpr u32 WS_FO = 0x5000;
constexpr u32 WS_OMG = 0x6000;
constexpr u32 WS_FH = 0x8000;
constexpr u32 WS_KVF = 0x20000;
constexpr u32 WS_KVP = 0x100000;
constexpr u32 WS_PQ = 0x300000;
constexpr u32 WS_HB = 0x800000;
constexpr u32 WS_OB = 0x1000000;
constexpr u32 WS_DBG = 0x1200000;
constexpr u32 WS_BYTES = 0x1300000;

constexpr u32 L_R0 = 0, L_R1 = 57344;
constexpr u32 L_Z0 = 16384, L_Z1 = 49152, L_Z2 = 81920, L_Z3 = 114688;
constexpr u32 L_WQ = 114688, L_PHK = 143360, L_MISC = 159744, L_V = L_R1;
constexpr u32 L_WPHI = L_MISC, L_GW = L_MISC + 2048, L_B1 = L_MISC + 3072, L_B2 = L_MISC + 3328, L_G = L_MISC + 3584;
constexpr u32 L_RED0 = 0, L_RED1 = L_WQ + 1024;
constexpr u32 LDS_BYTES = 163840;

constexpr u32 SPIN_CAP = 1u << 15;
constexpr float LOG2E = 1.4426950408889634f;

#define MFMA(a, b, c) __builtin_amdgcn_mfma_f32_32x32x16_bf16(__builtin_bit_cast(bf8, a), __builtin_bit_cast(bf8, b), c, 0, 0, 0)
#define SBAR() __builtin_amdgcn_sched_barrier(0)
#define WAITV(N) asm volatile("s_waitcnt vmcnt(%0)" ::"i"(N) : "memory")
#define WAITV_BAR(N) asm volatile("s_waitcnt vmcnt(%0) lgkmcnt(0)\n\ts_barrier" ::"i"(N) : "memory")
#define BAR() asm volatile("s_waitcnt lgkmcnt(0)\n\ts_barrier" ::: "memory")

#define STAMP(k)

DI u32x4 mksrd(const void* p, u32 bytes) {
  u64 a = (u64)p;
  return (u32x4){(u32)a, (u32)(a >> 32) & 0xffffu, bytes, 0x00020000u};
}
DI void dma(u32 m0v, u32 voff, u32x4 srd, u32 soff) {
  asm volatile("s_mov_b32 m0, %0\n\ts_nop 0\n\tbuffer_load_dwordx4 %1, %2, %3 offen lds" ::"s"(m0v), "v"(voff), "s"(srd), "s"(soff) : "memory");
}
template <int OFF> DI void ld16(u32x4& d, u32 voff, u32x4 srd, u32 soff) {
  asm volatile("buffer_load_dwordx4 %0, %1, %2, %3 offen offset:%4 sc1" : "=v"(d) : "v"(voff), "s"(srd), "s"(soff), "i"(OFF) : "memory");
}
DI void ld8(u32x2& d, u32 voff, u32x4 srd, u32 soff) {
  asm volatile("buffer_load_dwordx2 %0, %1, %2, %3 offen sc1" : "=v"(d) : "v"(voff), "s"(srd), "s"(soff) : "memory");
}
DI void ld4(u32& d, u32 voff, u32x4 srd, u32 soff) {
  asm volatile("buffer_load_dword %0, %1, %2, %3 offen sc1" : "=v"(d) : "v"(voff), "s"(srd), "s"(soff) : "memory");
}
template <int OFF> DI void st16(u32x4 d, u32 voff, u32x4 srd, u32 soff) {
  asm volatile("buffer_store_dwordx4 %0, %1, %2, %3 offen offset:%4 sc1\n\ts_nop 1" ::"v"(d), "v"(voff), "s"(srd), "s"(soff), "i"(OFF) : "memory");
}
DI void st8(u32x2 d, u32 voff, u32x4 srd, u32 soff) {
  asm volatile("buffer_store_dwordx2 %0, %1, %2, %3 offen sc1\n\ts_nop 1" ::"v"(d), "v"(voff), "s"(srd), "s"(soff) : "memory");
}
DI void st4(u32 d, u32 voff, u32x4 srd, u32 soff) {
  asm volatile("buffer_store_dword %0, %1, %2, %3 offen sc1\n\ts_nop 1" ::"v"(d), "v"(voff), "s"(srd), "s"(soff) : "memory");
}
template <int N> DI void waitr(u32x4& a) { asm volatile("s_waitcnt vmcnt(%1)" : "+v"(a) : "i"(N) : "memory"); }
template <int N> DI void waitr(u32x2& a) { asm volatile("s_waitcnt vmcnt(%1)" : "+v"(a) : "i"(N) : "memory"); }
DI void pin(u32x4& a) { asm volatile("" : "+v"(a)); }

DI void flag(u32x2 tagv, u32 flane, u32x4 srd, u32 soff) { st8(tagv, flane, srd, soff); }
DI u64 tagmask(u32x2 p, u32x2 tagv) { return __ballot(p.x == tagv.x && p.y == tagv.y); }
DI u64 spin(u64 m, u64 need, u32 voff, u32x4 srd, u32 soff, u32x2 tagv) {
  for (u32 it = 0; (m & need) != need && it < SPIN_CAP; ++it) {
    u32x2 p;
    ld8(p, voff, srd, soff);
    waitr<0>(p);
    m = tagmask(p, tagv);
  }
  return m;
}

DI u32 pk(float a, float b) { return __builtin_bit_cast(u32, __builtin_convertvector((f2){a, b}, bf2)); }
DI u32x4 pk8(const f16v& v, int o) { return (u32x4){pk(v[o], v[o + 1]), pk(v[o + 2], v[o + 3]), pk(v[o + 4], v[o + 5]), pk(v[o + 6], v[o + 7])}; }
DI u32x2 trd(LAS char* p) { return __builtin_bit_cast(u32x2, __builtin_amdgcn_ds_read_tr16_b64_v4i16((LAS s4*)p)); }
DI u32x4 tr2(LAS char* p, LAS char* q) { u32x2 a = trd(p), b = trd(q); return (u32x4){a.x, a.y, b.x, b.y}; }
DI float blo(u32 v) { return __uint_as_float(v << 16); }
DI float bhi(u32 v) { return __uint_as_float(v & 0xffff0000u); }
DI float xsum(float v) { auto r = __builtin_amdgcn_permlane32_swap(__float_as_uint(v), __float_as_uint(v), false, false); return __uint_as_float(r[0]) + __uint_as_float(r[1]); }
DI float xhi(float v) { auto r = __builtin_amdgcn_permlane32_swap(__float_as_uint(v), __float_as_uint(v), false, false); return __uint_as_float(r[1]); }
DI float mx3(float a, float b, float c) { return __builtin_fmaxf(__builtin_fmaxf(a, b), c); }
DI float mn3(float a, float b, float c) { return __builtin_fminf(__builtin_fminf(a, b), c); }
DI float dot2(u32 a, u32 b, float c) { return __builtin_amdgcn_fdot2_f32_bf16(__builtin_bit_cast(bf2, a), __builtin_bit_cast(bf2, b), c, false); }

DI f16v bias16(LAS char* p) {
  f16v a;
#pragma unroll
  for (int g = 0; g < 4; ++g) {
    u32x2 v = *(LAS u32x2*)(p + 16 * g);
    a[4 * g] = blo(v.x); a[4 * g + 1] = bhi(v.x); a[4 * g + 2] = blo(v.y); a[4 * g + 3] = bhi(v.y);
  }
  return a;
}
DI float gelu1(float x, float cg) {
  const float C0 = -2.0f * 0.7978845608028654f * LOG2E, C1 = C0 * 0.044715f;
  float t = __builtin_fmaf(x * x, C1, C0);
  float e = __builtin_amdgcn_exp2f(x * t);
  return x * __builtin_amdgcn_rcpf(__builtin_fmaf(e, cg, cg));
}
DI void featmap(const f16v& a, LAS char* wp, bool hi, f16v& ep, f16v& en) {
  u32x4 w0 = tr2(wp, wp + 8 * 56);
  u32x4 w1 = tr2(wp + 16 * 56, wp + 24 * 56);
  w1.z = hi ? 0u : w1.z; w1.w = hi ? 0u : w1.w;
  f16v p = {};
  p = MFMA(w0, pk8(a, 0), p);
  p = MFMA(w1, pk8(a, 8), p);
  float mx = mx3(p[0], p[1], p[2]), mn = mn3(p[0], p[1], p[2]);
#pragma unroll
  for (int i = 3; i < 15; i += 2) { mx = mx3(mx, p[i], p[i + 1]); mn = mn3(mn, p[i], p[i + 1]); }
  mx = __builtin_fmaxf(mx, p[15]); mn = __builtin_fminf(mn, p[15]);
  { auto r = __builtin_amdgcn_permlane32_swap(__float_as_uint(mx), __float_as_uint(mx), false, false); mx = __builtin_fmaxf(__uint_as_float(r[0]), __uint_as_float(r[1])); }
  { auto r = __builtin_amdgcn_permlane32_swap(__float_as_uint(mn), __float_as_uint(mn), false, false); mn = __builtin_fminf(__uint_as_float(r[0]), __uint_as_float(r[1])); }
  const float a0 = -mx * LOG2E, a1 = mn * LOG2E;
#pragma unroll
  for (int i = 0; i < 16; ++i) {
    ep[i] = __builtin_amdgcn_exp2f(__builtin_fmaf(p[i], LOG2E, a0));
    en[i] = __builtin_amdgcn_exp2f(__builtin_fmaf(p[i], -LOG2E, a1));
  }
}
DI float sum14(const f16v& e, bool hi) {
  float a = (e[0] + e[1]) + (e[2] + e[3]), b = (e[4] + e[5]) + (e[6] + e[7]), c = (e[8] + e[9]) + (e[10] + e[11]), d = (e[12] + e[13]) + (e[14] + e[15]);
  return xsum((a + b) + (c + (hi ? 0.f : d)));
}

__global__ __launch_bounds__(512, 2) void fused_kernel(const u16* __restrict__ x, const u16* __restrict__ wqkv, const u16* __restrict__ wphi,
                                                       const u16* __restrict__ wo, const u16* __restrict__ w1, const u16* __restrict__ b1,
                                                       const u16* __restrict__ w2, const u16* __restrict__ b2, const u16* __restrict__ gw,
                                                       u16* __restrict__ out, char* __restrict__ ws) {
  __shared__ __attribute__((aligned(16))) char smem_[LDS_BYTES];
  LAS char* sm = (LAS char*)smem_;
  const u32 lds0 = (u32)(size_t)sm;
  STAMP(0);
  const u32 tid = threadIdx.x, lane = tid & 63;
  const u32 w = __builtin_amdgcn_readfirstlane(tid >> 6), ch = w >> 2, tg = w & 3;
  const u32 b = blockIdx.x, xcd = b & 7, rr = b >> 3;
  const u32 t = (xcd & 1) * 8 + (rr & 7), hd = (xcd >> 1) * 4 + (rr >> 3);
  const bool special = ((hd & 3) == 3) && (((hd >> 2) & 1) == (t & 1));
  const u32 js = special ? 0u : ((t & 1) ? hd - (hd > 7) : hd - (hd > 3) - (hd > 11));
  const u32 nu = rr < 24 ? rr : 24 + ((rr & 7) >> 1);
  const u32 ut = xcd * 4 + (nu & 3), ct = nu >> 2;
  const u32 r = lane & 31, h = lane >> 5, grp = (lane >> 4) & 1, q = (lane >> 2) & 3, p = lane & 3;
  const bool hi = h != 0;

  const u32x4 sX = mksrd(x, 2048 * 896), sQKV = mksrd(wqkv, 448 * 2688), sW1 = mksrd(w1, special ? 0u : 448u * 3584u), sWS = mksrd(ws, WS_BYTES);

  u32x2 tagv;
  ld8(tagv, 0u, sWS, WS_EPOCH);
  if (w < 4) {
    const char* g;
    if (w == 0) g = (const char*)wphi + hd * 1568 + lane * 16;
    else if (w == 1) g = (const char*)wphi + hd * 1568 + 1024 + (lane < 33 ? lane : 33) * 16;
    else if (w == 2) g = (const char*)gw + (lane < 55 ? lane : 55) * 16;
    else g = (lane >= 16 && lane < 24) ? (const char*)b2 + ct * 128 + (lane - 16) * 16 : (const char*)b1 + js * 256 + (lane & 15) * 16;
    if (w < 3 || lane < 32)
      asm volatile("s_mov_b32 m0, %0\n\ts_nop 0\n\tglobal_load_lds_dwordx4 %1, off" ::"s"(lds0 + L_MISC + w * 1024), "v"(g) : "memory");
  }
  const u32 l8 = lane >> 3, c8 = lane & 7;
  const u32 vx0 = (t * 128 + 16 * w + l8) * 896 + ((c8 ^ (lane >> 4)) << 4);
  const u32 vx1 = (t * 128 + 16 * w + 8 + l8) * 896 + ((c8 ^ (4 + (lane >> 4))) << 4);
  const u32 c4 = p == 3 ? 40u : p * 16u;
  const u32 vq = (lane >> 2) * 2688 + hd * 56 + c4;
  const u32 vkv = vq + 16 * tg * 2688 + 896 * (1 + ch);
  const u32 sw8 = (c8 ^ ((l8 & 2) << 1)) << 4;
  const u32 vw1 = (8 * w + l8) * 3584 + js * 256 + sw8;
  const u32 mw = lds0 + w * 1024;

#define CHUNK(c, Z)                                           \
  do {                                                        \
    dma(lds0 + (Z) + w * 2048, vx0, sX, (c) * 128);           \
    dma(lds0 + (Z) + w * 2048 + 1024, vx1, sX, (c) * 128);    \
    dma(mw + (Z) + 16384, vkv, sQKV, (c) * 64 * 2688);        \
    dma(mw + (Z) + 24576, vw1, sW1, (c) * 64 * 3584);         \
  } while (0)
#define L1U(u) dma(mw + L_R0 + (u) * 8192, vw1, sW1, (u) * 64 * 3584 + 128)
  CHUNK(0, L_Z0); CHUNK(1, L_Z1); CHUNK(2, L_Z2); CHUNK(3, L_Z3);

  const u32 ax = (tg * 32 + r) * 128 + ((h ^ ((r >> 1) & 7)) << 4);
  const u32 sl = 4 * grp + p;
  const u32 a64 = (8 * h + q) * 64 + ((sl ^ (sl >= 6 ? 1u : 0u)) << 3);
  const u32 a128 = (8 * h + q) * 128 + ((ch ^ (q >> 1)) << 6) + grp * 32 + p * 8;
  const u32 flane = lane == 0 ? 0u : 0x80000000u;

  u32x4 xf[28];
  f16v accA = {}, accH, accH1;
  float gl = 0.f;
#define STEP(c, Z)                                                                                   \
  do {                                                                                               \
    _Pragma("unroll") for (int s = 0; s < 4; ++s) {                                                  \
      xf[4 * (c) + s] = *(LAS u32x4*)(sm + (Z) + (ax ^ (32 * s)));                                   \
      u32x4 gf = *(LAS u32x4*)(sm + L_GW + (c) * 128 + s * 32 + h * 16);                             \
      LAS char* pa = sm + (Z) + 16384 + ch * 4096 + s * 1024 + a64;                                  \
      LAS char* ph = sm + (Z) + 24576 + s * 2048 + a128;                                             \
      u32x4 wa = tr2(pa, pa + 256), wh = tr2(ph, ph + 512);                                          \
      accA = MFMA(wa, xf[4 * (c) + s], accA);                                                        \
      accH = MFMA(wh, xf[4 * (c) + s], accH);                                                        \
      _Pragma("unroll") for (int e = 0; e < 4; ++e) gl = dot2(xf[4 * (c) + s][e], gf[e], gl);        \
    }                                                                                                \
  } while (0)

  waitr<12>(tagv);
  BAR();
  STAMP(1);
  {
    u64 e = ((u64)__builtin_amdgcn_readfirstlane(tagv.y) << 32 | __builtin_amdgcn_readfirstlane(tagv.x)) + 0x9E3779B97F4A7C15ull;
    tagv = (u32x2){(u32)e, (u32)(e >> 32)};
  }
  accH = bias16(sm + L_B1 + ch * 64 + h * 8);
  STEP(0, L_Z0);
  WAITV_BAR(8); CHUNK(4, L_Z0); STEP(1, L_Z1);
  WAITV_BAR(8); CHUNK(5, L_Z1); STEP(2, L_Z2);
  WAITV_BAR(8); CHUNK(6, L_Z2); STEP(3, L_Z3);
  WAITV_BAR(8);
#pragma unroll
  for (int n = 0; n < 3; ++n) dma(mw + L_WQ + n * 8192, vq, sQKV, (n * 8 + w) * 16 * 2688);
  if (ch == 0) dma(mw + L_WQ + 3 * 8192, vq, sQKV, (24 + w) * 16 * 2688);
  STEP(4, L_Z0);
  WAITV_BAR(7); L1U(0); L1U(1); L1U(2); L1U(3); STEP(5, L_Z1);
  WAITV_BAR(7); L1U(4); L1U(5); L1U(6); STEP(6, L_Z2);
  STAMP(2);
  accH1 = bias16(sm + L_B1 + 128 + ch * 64 + h * 8);
  gl = xsum(gl);
  const float cg = __builtin_fminf(1.f + __builtin_amdgcn_exp2f(gl * LOG2E), 3.0e38f);

  const u32x4 sW2 = mksrd(w2, 1792 * 896);
  const u32 vb = l8 * 896 + ct * 128 + sw8;
  u32 rowoff[7];
#pragma unroll
  for (int u = 0; u < 7; ++u) { u32 m = 7 * tg + u; rowoff[u] = (128 * (m >> 1) + 16 * (m & 1)) * 896; }
  const u32 mb = lds0 + tg * 2048;
  const u32 l16 = lane * 16;
  const u32 atx = (tg * 32 + r) * 64 + ((h ^ ((r >> 1) & 7)) << 3);
  LAS char* wp = sm + L_WPHI + (4 * h + q) * 56 + (sl == 7 ? 48u : sl * 8);
  const u32 hbP = WS_HB + ((t * 4 + tg) * 4 * 28 + 2 * js) * 1024;
  const u32 fhP = WS_FH + (t * 4 + tg) * 512 + js * 8;

  if (ch == 0) {
    f16v ep, en;
    featmap(accA, wp, hi, ep, en);
    const float rp = __builtin_amdgcn_rcpf(sum14(ep, hi)), rn = __builtin_amdgcn_rcpf(sum14(en, hi));
#pragma unroll
    for (int g = 0; g < 4; ++g) {
      *(LAS u32x2*)(sm + L_PHK + (atx ^ (16 * g))) = (u32x2){pk(ep[4 * g] * rp, ep[4 * g + 1] * rp), pk(ep[4 * g + 2] * rp, ep[4 * g + 3] * rp)};
      *(LAS u32x2*)(sm + L_PHK + 8192 + (atx ^ (16 * g))) = (u32x2){pk(en[4 * g] * rn, en[4 * g + 1] * rn), pk(en[4 * g + 2] * rn, en[4 * g + 3] * rn)};
    }
    if (!hi) *(LAS float*)(sm + L_G + (tg * 32 + r) * 4) = gl;
    WAITV_BAR(7);
    STAMP(3);
    if (tg < 2) {
      const u32 a0 = (8 * h + q) * 64 + ((sl ^ (4 * h + (q >> 1))) << 3), a1 = (a0 ^ 16) + 256;
      f16v kv = {};
#pragma unroll
      for (int ks = 0; ks < 8; ++ks) {
        u32x4 fa = tr2(sm + L_PHK + tg * 8192 + ks * 1024 + a0, sm + L_PHK + tg * 8192 + ks * 1024 + a1);
        u32x4 fb = tr2(sm + L_V + ks * 1024 + a0, sm + L_V + ks * 1024 + a1);
        kv = MFMA(fa, fb, kv);
      }
      u32x4 o0 = __builtin_bit_cast(u32x4, (f4){kv[0], kv[1], kv[2], kv[3]}), o1 = __builtin_bit_cast(u32x4, (f4){kv[4], kv[5], kv[6], kv[7]});
      u32x4 o2 = __builtin_bit_cast(u32x4, (f4){kv[8], kv[9], kv[10], kv[11]}), o3 = __builtin_bit_cast(u32x4, (f4){kv[12], kv[13], kv[14], kv[15]});
      asm volatile("s_nop 7\n\ts_nop 4" : "+v"(o0), "+v"(o1), "+v"(o2), "+v"(o3));
      const u32 so = WS_KVP + ((hd * 2 + tg) * 16 + t) * 4096;
      st16<0>(o0, l16, sWS, so); st16<1024>(o1, l16, sWS, so); st16<2048>(o2, l16, sWS, so); st16<3072>(o3, l16, sWS, so);
    }
  } else {
    accA[12] = hi ? 1.0f : accA[12];
#pragma unroll
    for (int g = 0; g < 4; ++g)
      *(LAS u32x2*)(sm + L_V + (atx ^ (16 * g))) = (u32x2){pk(accA[4 * g], accA[4 * g + 1]), pk(accA[4 * g + 2], accA[4 * g + 3])};
  }
  if (!special) {
    const u32 vb0 = vb + 32 * 896, vb1 = vb0 + 8 * 896;
#pragma unroll
    for (int g = 0; g < 4; ++g) {
      if (ch == 0) {
#pragma unroll
        for (int n = 3 * g; n < 3 * g + 3; ++n) dma(mb + L_R1 + (1 + (n >> 1)) * 8192 + (n & 1) * 1024, (n & 1) ? vb1 : vb0, sW2, rowoff[1 + (n >> 1)]);
      }
#pragma unroll
      for (int e = 0; e < 4; ++e) accH[4 * g + e] = gelu1(accH[4 * g + e], cg);
      SBAR();
    }
    const u32 so = hbP + (1 - ch) * 28 * 1024;
    st16<0>(pk8(accH, 0), l16, sWS, so);
    st16<1024>(pk8(accH, 8), l16, sWS, so);
  }
  if (ch == 0) {
    if (special) WAITV(0); else WAITV(14);
    if (tg < 2) flag(tagv, flane, sWS, WS_FK + ((hd * 2 + tg) * 16 + t) * 8);
    if (!special) BAR();
    STAMP(4);
  } else {
    if (special) WAITV_BAR(7); else WAITV_BAR(9);
    STAMP(3);
    f16v aq = {};
#pragma unroll
    for (int s = 0; s < 28; ++s) {
      LAS char* pa = sm + L_WQ + s * 1024 + a64;
      aq = MFMA(tr2(pa, pa + 256), xf[s], aq);
    }
    if (!special) {
      WAITV(0);
      flag(tagv, flane, sWS, fhP);
      BAR();
      STAMP(4);
      dma(mb + L_R1, vb + 32 * 896, sW2, rowoff[0]);
      dma(mb + L_R1 + 1024, vb + 40 * 896, sW2, rowoff[0]);
    }
    f16v ep, en;
    featmap(aq, wp, hi, ep, en);
    const u32 so = WS_PQ + ((t * 16 + hd) * 4 + tg) * 4096;
    st16<0>(pk8(ep, 0), l16, sWS, so); st16<1024>(pk8(ep, 8), l16, sWS, so);
    st16<2048>(pk8(en, 0), l16, sWS, so); st16<3072>(pk8(en, 8), l16, sWS, so);
    if (!special) BAR();
    else { WAITV(0); flag(tagv, flane, sWS, WS_FQ + ((t * 16 + hd) * 4 + tg) * 8); }
  }

  if (special) {
    const u32 half = hd >> 3;
    if (ch == 0) {
      spin(0, 0xffffull, (lane & 15) * 8, sWS, WS_FK + (t * 2 + half) * 128, tagv);
      STAMP(5);
      u32x4 v[16];
      const u32 so = WS_KVP + (t * 2 + half) * 65536 + tg * 1024;
#pragma unroll
      for (int k = 0; k < 16; ++k) ld16<0>(v[k], l16, sWS, so + k * 4096);
      waitr<0>(v[0]);
#pragma unroll
      for (int k = 1; k < 16; ++k) pin(v[k]);
      f4 a = __builtin_bit_cast(f4, v[0]);
#pragma unroll
      for (int k = 1; k < 16; ++k) a += __builtin_bit_cast(f4, v[k]);
      u32x2 o = {pk(a[0], a[1]), pk(a[2], a[3])};
      if (r == 29) o = (u32x2){0x3f803f80u, 0x3f803f80u};
      if (hi && tg == 3) o = (u32x2){0u, 0u};
      st8(o, l16, sWS, WS_KVF + (t * 2 + half) * 2048 + (tg >> 1) * 1024 + (tg & 1) * 8);
      WAITV(0);
      flag(tagv, flane, sWS, WS_FF + t * 128 + (half * 4 + tg) * 8);
      STAMP(6);
    }
    const u32 oh = half * 8 + w;
    spin(0, 0xfull, (lane & 3) * 8, sWS, WS_FQ + (t * 16 + oh) * 32, tagv);
    u32x4 e[16];
    {
      const u32 so = WS_PQ + (t * 16 + oh) * 16384;
#pragma unroll
      for (int k = 0; k < 4; ++k) {
        ld16<0>(e[4 * k], l16, sWS, so + k * 4096); ld16<1024>(e[4 * k + 1], l16, sWS, so + k * 4096);
        ld16<2048>(e[4 * k + 2], l16, sWS, so + k * 4096); ld16<3072>(e[4 * k + 3], l16, sWS, so + k * 4096);
      }
    }
    WAITV(0);
#pragma unroll
    for (int k = 0; k < 16; ++k) pin(e[k]);
    STAMP(7);
    spin(0, 0xffull, (lane & 7) * 8, sWS, WS_FF + oh * 128, tagv);
    STAMP(8);
    u32x4 kf[4];
    ld16<0>(kf[0], l16, sWS, WS_KVF + oh * 4096); ld16<1024>(kf[1], l16, sWS, WS_KVF + oh * 4096);
    ld16<2048>(kf[2], l16, sWS, WS_KVF + oh * 4096); ld16<3072>(kf[3], l16, sWS, WS_KVF + oh * 4096);
    if (half == 0 && ch == 0 && !hi) st4(__float_as_uint(__builtin_amdgcn_rcpf(cg)), r * 4, sWS, WS_OMG + (t * 128 + tg * 32) * 4);
    WAITV(0);
    pin(kf[0]); pin(kf[1]); pin(kf[2]); pin(kf[3]);
#pragma unroll
    for (int k = 0; k < 4; ++k) {
      f16v ap = {}, an = {};
      ap = MFMA(kf[0], e[4 * k], ap); an = MFMA(kf[2], e[4 * k + 2], an);
      ap = MFMA(kf[1], e[4 * k + 1], ap); an = MFMA(kf[3], e[4 * k + 3], an);
      const float lg = *(LAS float*)(sm + L_G + (k * 32 + r) * 4);
      const float g = __builtin_amdgcn_rcpf(1.f + __builtin_amdgcn_exp2f(-lg * LOG2E));
      const float ip = __builtin_amdgcn_rcpf(ap[13]), in = __builtin_amdgcn_rcpf(an[13]);
      const float sc = g * __builtin_amdgcn_rcpf(ap[12] * ip + an[12] * in + 1e-6f);
      const float al = xhi(sc * ip), be = xhi(sc * in);
      f16v o;
#pragma unroll
      for (int i = 0; i < 16; ++i) o[i] = ap[i] * al + an[i] * be;
      u32x4 o0 = pk8(o, 0), o1 = pk8(o, 8);
      o1.z = hi ? 0u : o1.z; o1.w = hi ? 0u : o1.w;
      const u32 so = WS_OB + ((t * 4 + k) * 16 + oh) * 2048;
      st16<0>(o0, l16, sWS, so); st16<1024>(o1, l16, sWS, so);
    }
    WAITV(0);
    flag(tagv, flane, sWS, WS_FO + (t * 16 + oh) * 8);
    STAMP(9);
  } else {
    const u32 tgG = ut * 2 + ch;
    const u32 fhC = WS_FH + tgG * 512;
    u32x2 pl;
#pragma unroll
    for (int s = 0; s < 28; ++s) {
      LAS char* ph = sm + L_R0 + s * 2048 + a128;
      accH1 = MFMA(tr2(ph, ph + 512), xf[s], accH1);
      if (s == 18) { SBAR(); ld8(pl, lane * 8, sWS, fhC); SBAR(); }
    }
    waitr<0>(pl);
    flag(tagv, flane, sWS, ch == 0 ? fhP + 128 : WS_FQ + ((t * 16 + hd) * 4 + tg) * 8);
    BAR();
    STAMP(5);
    u64 fm = spin(tagmask(pl, tagv), 0x3fffull, lane * 8, sWS, fhC, tagv);
    u32x4 A0[7], A1[7], A2[7], A3[7], A4[8];
    const u32 hbC = WS_HB + (tgG * 4 * 28 + 7 * tg) * 1024;
#define LOADA(A, so)                                                                                                    \
  do {                                                                                                                  \
    ld16<0>(A[0], l16, sWS, (so)); ld16<1024>(A[1], l16, sWS, (so)); ld16<2048>(A[2], l16, sWS, (so));                  \
    ld16<3072>(A[3], l16, sWS, (so)); ld16<0>(A[4], l16, sWS, (so) + 4096); ld16<1024>(A[5], l16, sWS, (so) + 4096);    \
    ld16<2048>(A[6], l16, sWS, (so) + 4096);                                                                            \
  } while (0)
    LOADA(A0, hbC);
    STAMP(6);
#pragma unroll
    for (int e = 0; e < 16; ++e) accH1[e] = gelu1(accH1[e], cg);
    {
      const u32 so = hbP + (2 + ch) * 28 * 1024;
      st16<0>(pk8(accH1, 0), l16, sWS, so);
      st16<1024>(pk8(accH1, 8), l16, sWS, so);
    }
    if (ch == 0) {
      BAR();
      ld8(pl, lane * 8, sWS, fhC);
#pragma unroll
      for (int n = 0; n < 14; ++n) dma(mb + L_R0 + (n >> 1) * 8192 + (n & 1) * 1024, (n & 1) ? vb + 8 * 896 : vb, sW2, rowoff[n >> 1]);
      waitr<14>(pl);
      flag(tagv, flane, sWS, fhP + 256);
      fm = tagmask(pl, tagv);
    }
    fm = spin(fm, 0x3fffull << 16, lane * 8, sWS, fhC, tagv);
    LOADA(A1, hbC + 28 * 1024);
    STAMP(7);
    f16v acc0 = {}, acc1 = {};
    const u32 ab0 = tg * 2048 + (4 * h + q) * 128 + ((q >> 1) << 6) + grp * 32 + p * 8, ab1 = ab0 ^ 64;
#define SEG(A, R, N0)                                                   \
  do {                                                                  \
    _Pragma("unroll") for (int u = 0; u < 7; ++u) {                     \
      LAS char* p0 = sm + (R) + u * 8192 + ab0;                         \
      LAS char* p1 = sm + (R) + u * 8192 + ab1;                         \
      u32x4 f0 = tr2(p0, p0 + 1024), f1 = tr2(p1, p1 + 1024);           \
      asm volatile("s_waitcnt vmcnt(%1)" : "+v"(A[u]) : "i"((N0) + 6 - u)); \
      acc0 = MFMA(f0, A[u], acc0);                                      \
      acc1 = MFMA(f1, A[u], acc1);                                      \
    }                                                                   \
  } while (0)
    const u32 vbh = vb + ch * 8 * 896, mbh = mb + ch * 1024;
#define STAGEB(R, so) _Pragma("unroll") for (int u = 0; u < 7; ++u) dma(mbh + (R) + u * 8192, vbh + (so) * 896, sW2, rowoff[u])
    SEG(A0, L_R1, 9);
    WAITV(7);
    if (ch == 1) flag(tagv, flane, sWS, fhP + 384);
    BAR();
    STAMP(8);
    ld8(pl, lane * 8, sWS, fhC);
    STAGEB(L_R1, 64);
    waitr<7>(pl);
    fm = spin(tagmask(pl, tagv), 0x3fffull << 32, lane * 8, sWS, fhC, tagv);
    LOADA(A2, hbC + 2 * 28 * 1024);
    SEG(A1, L_R0, 14);
    WAITV_BAR(7);
    STAMP(9);
    ld8(pl, lane * 8, sWS, fhC);
    STAGEB(L_R0, 96);
    SEG(A2, L_R1, 8);
    waitr<7>(pl);
    fm = spin(tagmask(pl, tagv), 0x3fffull << 48, lane * 8, sWS, fhC, tagv);
    LOADA(A3, hbC + 3 * 28 * 1024);
    WAITV_BAR(7);
    STAMP(10);
    const u32 foC = WS_FO + (ut >> 1) * 128;
    ld8(pl, (lane & 15) * 8, sWS, foC);
    {
      const u32x4 sWO = mksrd(wo, 448 * 896);
#pragma unroll
      for (int u = 0; u < 7; ++u) dma(lds0 + L_R1 + tg * 14336 + ch * 1024 + u * 2048, vbh, sWO, (tg * 112 + u * 16) * 896);
    }
    SEG(A3, L_R0, 8);
    waitr<7>(pl);
    spin(tagmask(pl, tagv), 0xffffull, (lane & 15) * 8, sWS, foC, tagv);
    STAMP(11);
    u32 omg;
    ld4(omg, r * 4, sWS, WS_OMG + tgG * 128);
    {
      const u32 so = WS_OB + (tgG * 16 + 4 * tg) * 2048;
      LOADA(A4, so);
      ld16<3072>(A4[7], l16, sWS, so + 4096);
    }
    WAITV_BAR(9);
    STAMP(12);
    {
      const u32 ao0 = ab0 + tg * (14336 - 2048), ao1 = ao0 ^ 64;
#pragma unroll
      for (int u = 0; u < 8; ++u) {
        LAS char* p0 = sm + L_R1 + (28 * (u >> 1) + 16 * (u & 1)) * 128 + ao0;
        LAS char* p1 = sm + L_R1 + (28 * (u >> 1) + 16 * (u & 1)) * 128 + ao1;
        u32x4 f0 = tr2(p0, p0 + 1024), f1 = tr2(p1, p1 + 1024);
        asm volatile("s_waitcnt vmcnt(%1)" : "+v"(A4[u]) : "i"(7 - u));
        acc0 = MFMA(f0, A4[u], acc0);
        acc1 = MFMA(f1, A4[u], acc1);
      }
    }
    asm volatile("" : "+v"(omg));
    STAMP(13);
    LAS char* red = sm + (ch ? L_RED1 : L_RED0) + l16;
#pragma unroll
    for (int g = 0; g < 4; ++g) {
      *(LAS f4*)(red + (g >> 1) * 8192 + tg * 2048 + (g & 1) * 1024) = (f4){acc0[4 * g], acc0[4 * g + 1], acc0[4 * g + 2], acc0[4 * g + 3]};
      *(LAS f4*)(red + (2 + (g >> 1)) * 8192 + tg * 2048 + (g & 1) * 1024) = (f4){acc1[4 * g], acc1[4 * g + 1], acc1[4 * g + 2], acc1[4 * g + 3]};
    }
    const u32x2 bA = *(LAS u32x2*)(sm + L_B2 + tg * 32 + h * 8), bB = *(LAS u32x2*)(sm + L_B2 + tg * 32 + 16 + h * 8);
    BAR();
    const float om = __uint_as_float(omg);
    f4 sa = (f4){blo(bA.x), bhi(bA.x), blo(bA.y), bhi(bA.y)} * om, sb = (f4){blo(bB.x), bhi(bB.x), blo(bB.y), bhi(bB.y)} * om;
#pragma unroll
    for (int k = 0; k < 4; ++k) {
      sa += *(LAS f4*)(red + tg * 8192 + k * 2048);
      sb += *(LAS f4*)(red + tg * 8192 + k * 2048 + 1024);
    }
    auto s0 = __builtin_amdgcn_permlane32_swap(pk(sa[0], sa[1]), pk(sb[0], sb[1]), false, false);
    auto s1 = __builtin_amdgcn_permlane32_swap(pk(sa[2], sa[3]), pk(sb[2], sb[3]), false, false);
    { u32x4 ov = (u32x4){s0[0], s1[0], s0[1], s1[1]}; char* op = (char*)out + (tgG * 32 + r) * 896 + ct * 128 + tg * 32 + h * 16;
      asm volatile("global_store_dwordx4 %0, %1, off sc1\n\ts_nop 1" :: "v"(op), "v"(ov) : "memory"); }
    if (b == 0 && w == 0) flag(tagv, flane, sWS, WS_EPOCH);
    STAMP(14);
  }
}

extern "C" void kernel_launch(void* const* d_in, const int* in_sizes, int n_in, void* d_out, int out_size, void* d_ws, size_t ws_size,
                              hipStream_t stream) {
  fused_kernel<<<256, 512, 0, stream>>>((const u16*)d_in[0], (const u16*)d_in[1], (const u16*)d_in[2], (const u16*)d_in[3], (const u16*)d_in[5],
                                        (const u16*)d_in[6], (const u16*)d_in[7], (const u16*)d_in[8], (const u16*)d_in[9], (u16*)d_out,
                                        (char*)d_ws);
}
```

```cpp
#include <hip/hip_runtime.h>
#include <stdint.h>
#define STAMPS 1

typedef unsigned u32;
typedef unsigned long long u64;
typedef unsigned short u16;
typedef __bf16 bf2 __attribute__((ext_vector_type(2)));
typedef __bf16 bf8 __attribute__((ext_vector_type(8)));
typedef short s4 __attribute__((ext_vector_type(4)));
typedef short s8 __attribute__((ext_vector_type(8)));
typedef float f2 __attribute__((ext_vector_type(2)));
typedef float f4 __attribute__((ext_vector_type(4)));
typedef float f16v __attribute__((ext_vector_type(16)));
typedef u32 u32x2 __attribute__((ext_vector_type(2)));
typedef u32 u32x4 __attribute__((ext_vector_type(4)));
#define LAS __attribute__((address_space(3)))
#define DI __device__ __forceinline__

constexpr u32 WS_EPOCH = 0;
constexpr u32 WS_FK = 0x1000;
constexpr u32 WS_FQ = 0x2000;
constexpr u32 WS_FF = 0x4000;
constexpr u32 WS_FO = 0x5000;
constexpr u32 WS_OMG = 0x6000;
constexpr u32 WS_FH = 0x8000;
constexpr u32 WS_KVF = 0x20000;
constexpr u32 WS_KVP = 0x100000;
constexpr u32 WS_PQ = 0x300000;
constexpr u32 WS_HB = 0x800000;
constexpr u32 WS_OB = 0x1000000;
constexpr u32 WS_DBG = 0x1200000;
constexpr u32 WS_BYTES = 0x1300000;

constexpr u32 L_R0 = 0, L_R1 = 57344;
constexpr u32 L_Z0 = 16384, L_Z1 = 49152, L_Z2 = 81920, L_Z3 = 114688;
constexpr u32 L_WQ = 114688, L_PHK = 143360, L_MISC = 159744, L_V = L_R1;
constexpr u32 L_WPHI = L_MISC, L_GW = L_MISC + 2048, L_B1 = L_MISC + 3072, L_B2 = L_MISC + 3328, L_G = L_MISC + 3584;
constexpr u32 L_RED0 = 0, L_RED1 = L_WQ + 1024;
constexpr u32 LDS_BYTES = 163840;

constexpr u32 SPIN_CAP = 1u << 15;
constexpr float LOG2E = 1.4426950408889634f;

#define MFMA(a, b, c) __builtin_amdgcn_mfma_f32_32x32x16_bf16(__builtin_bit_cast(bf8, a), __builtin_bit_cast(bf8, b), c, 0, 0, 0)
#define SBAR() __builtin_amdgcn_sched_barrier(0)
#define WAITV(N) asm volatile("s_waitcnt vmcnt(%0)" ::"i"(N) : "memory")
#define WAITV_BAR(N) asm volatile("s_waitcnt vmcnt(%0) lgkmcnt(0)\n\ts_barrier" ::"i"(N) : "memory")
#define BAR() asm volatile("s_waitcnt lgkmcnt(0)\n\ts_barrier" ::: "memory")

#define STAMP(k) do { u64 t_; asm volatile("s_memrealtime %0\n\ts_waitcnt lgkmcnt(0)" : "=s"(t_) :: "memory"); st[k] = (u32)t_; } while (0)

DI u32x4 mksrd(const void* p, u32 bytes) {
  u64 a = (u64)p;
  return (u32x4){(u32)a, (u32)(a >> 32) & 0xffffu, bytes, 0x00020000u};
}
DI void dma(u32 m0v, u32 voff, u32x4 srd, u32 soff) {
  asm volatile("s_mov_b32 m0, %0\n\ts_nop 0\n\tbuffer_load_dwordx4 %1, %2, %3 offen lds" ::"s"(m0v), "v"(voff), "s"(srd), "s"(soff) : "memory");
}
template <int OFF> DI void ld16(u32x4& d, u32 voff, u32x4 srd, u32 soff) {
  asm volatile("buffer_load_dwordx4 %0, %1, %2, %3 offen offset:%4 sc1" : "=v"(d) : "v"(voff), "s"(srd), "s"(soff), "i"(OFF) : "memory");
}
DI void ld8(u32x2& d, u32 voff, u32x4 srd, u32 soff) {
  asm volatile("buffer_load_dwordx2 %0, %1, %2, %3 offen sc1" : "=v"(d) : "v"(voff), "s"(srd), "s"(soff) : "memory");
}
DI void ld4(u32& d, u32 voff, u32x4 srd, u32 soff) {
  asm volatile("buffer_load_dword %0, %1, %2, %3 offen sc1" : "=v"(d) : "v"(voff), "s"(srd), "s"(soff) : "memory");
}
template <int OFF> DI void st16(u32x4 d, u32 voff, u32x4 srd, u32 soff) {
  asm volatile("buffer_store_dwordx4 %0, %1, %2, %3 offen offset:%4 sc1\n\ts_nop 1" ::"v"(d), "v"(voff), "s"(srd), "s"(soff), "i"(OFF) : "memory");
}
DI void st8(u32x2 d, u32 voff, u32x4 srd, u32 soff) {
  asm volatile("buffer_store_dwordx2 %0, %1, %2, %3 offen sc1\n\ts_nop 1" ::"v"(d), "v"(voff), "s"(srd), "s"(soff) : "memory");
}
DI void st4(u32 d, u32 voff, u32x4 srd, u32 soff) {
  asm volatile("buffer_store_dword %0, %1, %2, %3 offen sc1\n\ts_nop 1" ::"v"(d), "v"(voff), "s"(srd), "s"(soff) : "memory");
}
template <int N> DI void waitr(u32x4& a) { asm volatile("s_waitcnt vmcnt(%1)" : "+v"(a) : "i"(N) : "memory"); }
template <int N> DI void waitr(u32x2& a) { asm volatile("s_waitcnt vmcnt(%1)" : "+v"(a) : "i"(N) : "memory"); }
DI void pin(u32x4& a) { asm volatile("" : "+v"(a)); }

DI void flag(u32x2 tagv, u32 flane, u32x4 srd, u32 soff) { st8(tagv, flane, srd, soff); }
DI u64 tagmask(u32x2 p, u32x2 tagv) { return __ballot(p.x == tagv.x && p.y == tagv.y); }
DI u64 spin(u64 m, u64 need, u32 voff, u32x4 srd, u32 soff, u32x2 tagv) {
  for (u32 it = 0; (m & need) != need && it < SPIN_CAP; ++it) {
    u32x2 p;
    ld8(p, voff, srd, soff);
    waitr<0>(p);
    m = tagmask(p, tagv);
  }
  return m;
}

DI u32 pk(float a, float b) { return __builtin_bit_cast(u32, __builtin_convertvector((f2){a, b}, bf2)); }
DI u32x4 pk8(const f16v& v, int o) { return (u32x4){pk(v[o], v[o + 1]), pk(v[o + 2], v[o + 3]), pk(v[o + 4], v[o + 5]), pk(v[o + 6], v[o + 7])}; }
DI u32x2 trd(LAS char* p) { return __builtin_bit_cast(u32x2, __builtin_amdgcn_ds_read_tr16_b64_v4i16((LAS s4*)p)); }
DI u32x4 tr2(LAS char* p, LAS char* q) { u32x2 a = trd(p), b = trd(q); return (u32x4){a.x, a.y, b.x, b.y}; }
DI float blo(u32 v) { return __uint_as_float(v << 16); }
DI float bhi(u32 v) { return __uint_as_float(v & 0xffff0000u); }
DI float xsum(float v) { auto r = __builtin_amdgcn_permlane32_swap(__float_as_uint(v), __float_as_uint(v), false, false); return __uint_as_float(r[0]) + __uint_as_float(r[1]); }
DI float xhi(float v) { auto r = __builtin_amdgcn_permlane32_swap(__float_as_uint(v), __float_as_uint(v), false, false); return __uint_as_float(r[1]); }
DI float mx3(float a, float b, float c) { return __builtin_fmaxf(__builtin_fmaxf(a, b), c); }
DI float mn3(float a, float b, float c) { return __builtin_fminf(__builtin_fminf(a, b), c); }
DI float dot2(u32 a, u32 b, float c) { return __builtin_amdgcn_fdot2_f32_bf16(__builtin_bit_cast(bf2, a), __builtin_bit_cast(bf2, b), c, false); }

DI f16v bias16(LAS char* p) {
  f16v a;
#pragma unroll
  for (int g = 0; g < 4; ++g) {
    u32x2 v = *(LAS u32x2*)(p + 16 * g);
    a[4 * g] = blo(v.x); a[4 * g + 1] = bhi(v.x); a[4 * g + 2] = blo(v.y); a[4 * g + 3] = bhi(v.y);
  }
  return a;
}
DI float gelu1(float x, float cg) {
  const float C0 = -2.0f * 0.7978845608028654f * LOG2E, C1 = C0 * 0.044715f;
  float t = __builtin_fmaf(x * x, C1, C0);
  float e = __builtin_amdgcn_exp2f(x * t);
  return x * __builtin_amdgcn_rcpf(__builtin_fmaf(e, cg, cg));
}
DI void featmap(const f16v& a, LAS char* wp, bool hi, f16v& ep, f16v& en) {
  u32x4 w0 = tr2(wp, wp + 8 * 56);
  u32x4 w1 = tr2(wp + 16 * 56, wp + 24 * 56);
  w1.z = hi ? 0u : w1.z; w1.w = hi ? 0u : w1.w;
  f16v p = {};
  p = MFMA(w0, pk8(a, 0), p);
  p = MFMA(w1, pk8(a, 8), p);
  float mx = mx3(p[0], p[1], p[2]), mn = mn3(p[0], p[1], p[2]);
#pragma unroll
  for (int i = 3; i < 15; i += 2) { mx = mx3(mx, p[i], p[i + 1]); mn = mn3(mn, p[i], p[i + 1]); }
  mx = __builtin_fmaxf(mx, p[15]); mn = __builtin_fminf(mn, p[15]);
  { auto r = __builtin_amdgcn_permlane32_swap(__float_as_uint(mx), __float_as_uint(mx), false, false); mx = __builtin_fmaxf(__uint_as_float(r[0]), __uint_as_float(r[1])); }
  { auto r = __builtin_amdgcn_permlane32_swap(__float_as_uint(mn), __float_as_uint(mn), false, false); mn = __builtin_fminf(__uint_as_float(r[0]), __uint_as_float(r[1])); }
  const float a0 = -mx * LOG2E, a1 = mn * LOG2E;
#pragma unroll
  for (int i = 0; i < 16; ++i) {
    ep[i] = __builtin_amdgcn_exp2f(__builtin_fmaf(p[i], LOG2E, a0));
    en[i] = __builtin_amdgcn_exp2f(__builtin_fmaf(p[i], -LOG2E, a1));
  }
}
DI float sum14(const f16v& e, bool hi) {
  float a = (e[0] + e[1]) + (e[2] + e[3]), b = (e[4] + e[5]) + (e[6] + e[7]), c = (e[8] + e[9]) + (e[10] + e[11]), d = (e[12] + e[13]) + (e[14] + e[15]);
  return xsum((a + b) + (c + (hi ? 0.f : d)));
}

__global__ __launch_bounds__(512, 2) void fused_kernel(const u16* __restrict__ x, const u16* __restrict__ wqkv, const u16* __restrict__ wphi,
                                                       const u16* __restrict__ wo, const u16* __restrict__ w1, const u16* __restrict__ b1,
                                                       const u16* __restrict__ w2, const u16* __restrict__ b2, const u16* __restrict__ gw,
                                                       u16* __restrict__ out, char* __restrict__ ws) {
  __shared__ __attribute__((aligned(16))) char smem_[LDS_BYTES];
  LAS char* sm = (LAS char*)smem_;
  const u32 lds0 = (u32)(size_t)sm;
  u32 st[16];
#pragma unroll
  for (int k = 0; k < 16; ++k) st[k] = 0;
  STAMP(0);
  u64 c0_ = __builtin_amdgcn_s_memtime();
  const u32 tid = threadIdx.x, lane = tid & 63;
  const u32 w = __builtin_amdgcn_readfirstlane(tid >> 6), ch = w >> 2, tg = w & 3;
  const u32 b = blockIdx.x, xcd = b & 7, rr = b >> 3;
  const u32 t = (xcd & 1) * 8 + (rr & 7), hd = (xcd >> 1) * 4 + (rr >> 3);
  const bool special = ((hd & 3) == 3) && (((hd >> 2) & 1) == (t & 1));
  const u32 js = special ? 0u : ((t & 1) ? hd - (hd > 7) : hd - (hd > 3) - (hd > 11));
  const u32 nu = rr < 24 ? rr : 24 + ((rr & 7) >> 1);
  const u32 ut = xcd * 4 + (nu & 3), ct = nu >> 2;
  const u32 r = lane & 31, h = lane >> 5, grp = (lane >> 4) & 1, q = (lane >> 2) & 3, p = lane & 3;
  const bool hi = h != 0;

  const u32x4 sX = mksrd(x, 2048 * 896), sQKV = mksrd(wqkv, 448 * 2688), sW1 = mksrd(w1, special ? 0u : 448u * 3584u), sWS = mksrd(ws, WS_BYTES);

  u32x2 tagv;
  ld8(tagv, 0u, sWS, WS_EPOCH);
  if (w < 4) {
    const char* g;
    if (w == 0) g = (const char*)wphi + hd * 1568 + lane * 16;
    else if (w == 1) g = (const char*)wphi + hd * 1568 + 1024 + (lane < 33 ? lane : 33) * 16;
    else if (w == 2) g = (const char*)gw + (lane < 55 ? lane : 55) * 16;
    else g = (lane >= 16 && lane < 24) ? (const char*)b2 + ct * 128 + (lane - 16) * 16 : (const char*)b1 + js * 256 + (lane & 15) * 16;
    if (w < 3 || lane < 32)
      asm volatile("s_mov_b32 m0, %0\n\ts_nop 0\n\tglobal_load_lds_dwordx4 %1, off" ::"s"(lds0 + L_MISC + w * 1024), "v"(g) : "memory");
  }
  const u32 l8 = lane >> 3, c8 = lane & 7;
  const u32 vx0 = (t * 128 + 16 * w + l8) * 896 + ((c8 ^ (lane >> 4)) << 4);
  const u32 vx1 = (t * 128 + 16 * w + 8 + l8) * 896 + ((c8 ^ (4 + (lane >> 4))) << 4);
  const u32 c4 = p == 3 ? 40u : p * 16u;
  const u32 vq = (lane >> 2) * 2688 + hd * 56 + c4;
  const u32 vkv = vq + 16 * tg * 2688 + 896 * (1 + ch);
  const u32 sw8 = (c8 ^ ((l8 & 2) << 1)) << 4;
  const u32 vw1 = (8 * w + l8) * 3584 + js * 256 + sw8;
  const u32 mw = lds0 + w * 1024;

#define CHUNK(c, Z)                                           \
  do {                                                        \
    dma(lds0 + (Z) + w * 2048, vx0, sX, (c) * 128);           \
    dma(lds0 + (Z) + w * 2048 + 1024, vx1, sX, (c) * 128);    \
    dma(mw + (Z) + 16384, vkv, sQKV, (c) * 64 * 2688);        \
    dma(mw + (Z) + 24576, vw1, sW1, (c) * 64 * 3584);         \
  } while (0)
#define L1U(u) dma(mw + L_R0 + (u) * 8192, vw1, sW1, (u) * 64 * 3584 + 128)
  CHUNK(0, L_Z0); CHUNK(1, L_Z1); CHUNK(2, L_Z2); CHUNK(3, L_Z3);

  const u32 ax = (tg * 32 + r) * 128 + ((h ^ ((r >> 1) & 7)) << 4);
  const u32 sl = 4 * grp + p;
  const u32 a64 = (8 * h + q) * 64 + ((sl ^ (sl >= 6 ? 1u : 0u)) << 3);
  const u32 a128 = (8 * h + q) * 128 + ((ch ^ (q >> 1)) << 6) + grp * 32 + p * 8;
  const u32 flane = lane == 0 ? 0u : 0x80000000u;

  u32x4 xf[28];
  f16v accA = {}, accH, accH1;
  float gl = 0.f;
#define STEP(c, Z)                                                                                   \
  do {                                                                                               \
    _Pragma("unroll") for (int s = 0; s < 4; ++s) {                                                  \
      xf[4 * (c) + s] = *(LAS u32x4*)(sm + (Z) + (ax ^ (32 * s)));                                   \
      u32x4 gf = *(LAS u32x4*)(sm + L_GW + (c) * 128 + s * 32 + h * 16);                             \
      LAS char* pa = sm + (Z) + 16384 + ch * 4096 + s * 1024 + a64;                                  \
      LAS char* ph = sm + (Z) + 24576 + s * 2048 + a128;                                             \
      u32x4 wa = tr2(pa, pa + 256), wh = tr2(ph, ph + 512);                                          \
      accA = MFMA(wa, xf[4 * (c) + s], accA);                                                        \
      accH = MFMA(wh, xf[4 * (c) + s], accH);                                                        \
      _Pragma("unroll") for (int e = 0; e < 4; ++e) gl = dot2(xf[4 * (c) + s][e], gf[e], gl);        \
    }                                                                                                \
  } while (0)

  waitr<12>(tagv);
  BAR();
  STAMP(1);
  {
    u64 e = ((u64)__builtin_amdgcn_readfirstlane(tagv.y) << 32 | __builtin_amdgcn_readfirstlane(tagv.x)) + 0x9E3779B97F4A7C15ull;
    tagv = (u32x2){(u32)e, (u32)(e >> 32)};
  }
  accH = bias16(sm + L_B1 + ch * 64 + h * 8);
  STEP(0, L_Z0);
  WAITV_BAR(8); CHUNK(4, L_Z0); STEP(1, L_Z1);
  WAITV_BAR(8); CHUNK(5, L_Z1); STEP(2, L_Z2);
  WAITV_BAR(8); CHUNK(6, L_Z2); STEP(3, L_Z3);
  WAITV_BAR(8);
#pragma unroll
  for (int n = 0; n < 3; ++n) dma(mw + L_WQ + n * 8192, vq, sQKV, (n * 8 + w) * 16 * 2688);
  if (ch == 0) dma(mw + L_WQ + 3 * 8192, vq, sQKV, (24 + w) * 16 * 2688);
  STEP(4, L_Z0);
  WAITV_BAR(7); L1U(0); L1U(1); L1U(2); L1U(3); STEP(5, L_Z1);
  WAITV_BAR(7); L1U(4); L1U(5); L1U(6); STEP(6, L_Z2);
  STAMP(2);
  accH1 = bias16(sm + L_B1 + 128 + ch * 64 + h * 8);
  gl = xsum(gl);
  const float cg = __builtin_fminf(1.f + __builtin_amdgcn_exp2f(gl * LOG2E), 3.0e38f);

  const u32x4 sW2 = mksrd(w2, 1792 * 896);
  const u32 vb = l8 * 896 + ct * 128 + sw8;
  u32 rowoff[7];
#pragma unroll
  for (int u = 0; u < 7; ++u) { u32 m = 7 * tg + u; rowoff[u] = (128 * (m >> 1) + 16 * (m & 1)) * 896; }
  const u32 mb = lds0 + tg * 2048;
  const u32 l16 = lane * 16;
  const u32 atx = (tg * 32 + r) * 64 + ((h ^ ((r >> 1) & 7)) << 3);
  LAS char* wp = sm + L_WPHI + (4 * h + q) * 56 + (sl == 7 ? 48u : sl * 8);
  const u32 hbP = WS_HB + ((t * 4 + tg) * 4 * 28 + 2 * js) * 1024;
  const u32 fhP = WS_FH + (t * 4 + tg) * 512 + js * 8;

  if (ch == 0) {
    f16v ep, en;
    featmap(accA, wp, hi, ep, en);
    const float rp = __builtin_amdgcn_rcpf(sum14(ep, hi)), rn = __builtin_amdgcn_rcpf(sum14(en, hi));
#pragma unroll
    for (int g = 0; g < 4; ++g) {
      *(LAS u32x2*)(sm + L_PHK + (atx ^ (16 * g))) = (u32x2){pk(ep[4 * g] * rp, ep[4 * g + 1] * rp), pk(ep[4 * g + 2] * rp, ep[4 * g + 3] * rp)};
      *(LAS u32x2*)(sm + L_PHK + 8192 + (atx ^ (16 * g))) = (u32x2){pk(en[4 * g] * rn, en[4 * g + 1] * rn), pk(en[4 * g + 2] * rn, en[4 * g + 3] * rn)};
    }
    if (!hi) *(LAS float*)(sm + L_G + (tg * 32 + r) * 4) = gl;
    WAITV_BAR(7);
    STAMP(3);
    if (tg < 2) {
      const u32 a0 = (8 * h + q) * 64 + ((sl ^ (4 * h + (q >> 1))) << 3), a1 = (a0 ^ 16) + 256;
      f16v kv = {};
#pragma unroll
      for (int ks = 0; ks < 8; ++ks) {
        u32x4 fa = tr2(sm + L_PHK + tg * 8192 + ks * 1024 + a0, sm + L_PHK + tg * 8192 + ks * 1024 + a1);
        u32x4 fb = tr2(sm + L_V + ks * 1024 + a0, sm + L_V + ks * 1024 + a1);
        kv = MFMA(fa, fb, kv);
      }
      u32x4 o0 = __builtin_bit_cast(u32x4, (f4){kv[0], kv[1], kv[2], kv[3]}), o1 = __builtin_bit_cast(u32x4, (f4){kv[4], kv[5], kv[6], kv[7]});
      u32x4 o2 = __builtin_bit_cast(u32x4, (f4){kv[8], kv[9], kv[10], kv[11]}), o3 = __builtin_bit_cast(u32x4, (f4){kv[12], kv[13], kv[14], kv[15]});
      asm volatile("s_nop 7\n\ts_nop 4" : "+v"(o0), "+v"(o1), "+v"(o2), "+v"(o3));
      const u32 so = WS_KVP + ((hd * 2 + tg) * 16 + t) * 4096;
      st16<0>(o0, l16, sWS, so); st16<1024>(o1, l16, sWS, so); st16<2048>(o2, l16, sWS, so); st16<3072>(o3, l16, sWS, so);
    }
  } else {
    accA[12] = hi ? 1.0f : accA[12];
#pragma unroll
    for (int g = 0; g < 4; ++g)
      *(LAS u32x2*)(sm + L_V + (atx ^ (16 * g))) = (u32x2){pk(accA[4 * g], accA[4 * g + 1]), pk(accA[4 * g + 2], accA[4 * g + 3])};
  }
  if (!special) {
    const u32 vb0 = vb + 32 * 896, vb1 = vb0 + 8 * 896;
#pragma unroll
    for (int g = 0; g < 4; ++g) {
      if (ch == 0) {
#pragma unroll
        for (int n = 3 * g; n < 3 * g + 3; ++n) dma(mb + L_R1 + (1 + (n >> 1)) * 8192 + (n & 1) * 1024, (n & 1) ? vb1 : vb0, sW2, rowoff[1 + (n >> 1)]);
      }
#pragma unroll
      for (int e = 0; e < 4; ++e) accH[4 * g + e] = gelu1(accH[4 * g + e], cg);
      SBAR();
    }
    const u32 so = hbP + (1 - ch) * 28 * 1024;
    st16<0>(pk8(accH, 0), l16, sWS, so);
    st16<1024>(pk8(accH, 8), l16, sWS, so);
  }
  if (ch == 0) {
    if (special) WAITV(0); else WAITV(14);
    if (tg < 2) flag(tagv, flane, sWS, WS_FK + ((hd * 2 + tg) * 16 + t) * 8);
    if (!special) BAR();
    STAMP(4);
  } else {
    if (special) WAITV_BAR(7); else WAITV_BAR(9);
    STAMP(3);
    f16v aq = {};
#pragma unroll
    for (int s = 0; s < 28; ++s) {
      LAS char* pa = sm + L_WQ + s * 1024 + a64;
      aq = MFMA(tr2(pa, pa + 256), xf[s], aq);
    }
    if (!special) {
      WAITV(0);
      flag(tagv, flane, sWS, fhP);
      BAR();
      STAMP(4);
      dma(mb + L_R1, vb + 32 * 896, sW2, rowoff[0]);
      dma(mb + L_R1 + 1024, vb + 40 * 896, sW2, rowoff[0]);
    }
    f16v ep, en;
    featmap(aq, wp, hi, ep, en);
    const u32 so = WS_PQ + ((t * 16 + hd) * 4 + tg) * 4096;
    st16<0>(pk8(ep, 0), l16, sWS, so); st16<1024>(pk8(ep, 8), l16, sWS, so);
    st16<2048>(pk8(en, 0), l16, sWS, so); st16<3072>(pk8(en, 8), l16, sWS, so);
    if (!special) BAR();
    else { WAITV(0); flag(tagv, flane, sWS, WS_FQ + ((t * 16 + hd) * 4 + tg) * 8); }
  }

  if (special) {
    const u32 half = hd >> 3;
    if (ch == 0) {
      spin(0, 0xffffull, (lane & 15) * 8, sWS, WS_FK + (t * 2 + half) * 128, tagv);
      STAMP(5);
      u32x4 v[16];
      const u32 so = WS_KVP + (t * 2 + half) * 65536 + tg * 1024;
#pragma unroll
      for (int k = 0; k < 16; ++k) ld16<0>(v[k], l16, sWS, so + k * 4096);
      waitr<0>(v[0]);
#pragma unroll
      for (int k = 1; k < 16; ++k) pin(v[k]);
      f4 a = __builtin_bit_cast(f4, v[0]);
#pragma unroll
      for (int k = 1; k < 16; ++k) a += __builtin_bit_cast(f4, v[k]);
      u32x2 o = {pk(a[0], a[1]), pk(a[2], a[3])};
      if (r == 29) o = (u32x2){0x3f803f80u, 0x3f803f80u};
      if (hi && tg == 3) o = (u32x2){0u, 0u};
      st8(o, l16, sWS, WS_KVF + (t * 2 + half) * 2048 + (tg >> 1) * 1024 + (tg & 1) * 8);
      WAITV(0);
      flag(tagv, flane, sWS, WS_FF + t * 128 + (half * 4 + tg) * 8);
      STAMP(6);
    }
    const u32 oh = half * 8 + w;
    spin(0, 0xfull, (lane & 3) * 8, sWS, WS_FQ + (t * 16 + oh) * 32, tagv);
    u32x4 e[16];
    {
      const u32 so = WS_PQ + (t * 16 + oh) * 16384;
#pragma unroll
      for (int k = 0; k < 4; ++k) {
        ld16<0>(e[4 * k], l16, sWS, so + k * 4096); ld16<1024>(e[4 * k + 1], l16, sWS, so + k * 4096);
        ld16<2048>(e[4 * k + 2], l16, sWS, so + k * 4096); ld16<3072>(e[4 * k + 3], l16, sWS, so + k * 4096);
      }
    }
    WAITV(0);
#pragma unroll
    for (int k = 0; k < 16; ++k) pin(e[k]);
    STAMP(7);
    spin(0, 0xffull, (lane & 7) * 8, sWS, WS_FF + oh * 128, tagv);
    STAMP(8);
    u32x4 kf[4];
    ld16<0>(kf[0], l16, sWS, WS_KVF + oh * 4096); ld16<1024>(kf[1], l16, sWS, WS_KVF + oh * 4096);
    ld16<2048>(kf[2], l16, sWS, WS_KVF + oh * 4096); ld16<3072>(kf[3], l16, sWS, WS_KVF + oh * 4096);
    if (half == 0 && ch == 0 && !hi) st4(__float_as_uint(__builtin_amdgcn_rcpf(cg)), r * 4, sWS, WS_OMG + (t * 128 + tg * 32) * 4);
    WAITV(0);
    pin(kf[0]); pin(kf[1]); pin(kf[2]); pin(kf[3]);
#pragma unroll
    for (int k = 0; k < 4; ++k) {
      f16v ap = {}, an = {};
      ap = MFMA(kf[0], e[4 * k], ap); an = MFMA(kf[2], e[4 * k + 2], an);
      ap = MFMA(kf[1], e[4 * k + 1], ap); an = MFMA(kf[3], e[4 * k + 3], an);
      const float lg = *(LAS float*)(sm + L_G + (k * 32 + r) * 4);
      const float g = __builtin_amdgcn_rcpf(1.f + __builtin_amdgcn_exp2f(-lg * LOG2E));
      const float ip = __builtin_amdgcn_rcpf(ap[13]), in = __builtin_amdgcn_rcpf(an[13]);
      const float sc = g * __builtin_amdgcn_rcpf(ap[12] * ip + an[12] * in + 1e-6f);
      const float al = xhi(sc * ip), be = xhi(sc * in);
      f16v o;
#pragma unroll
      for (int i = 0; i < 16; ++i) o[i] = ap[i] * al + an[i] * be;
      u32x4 o0 = pk8(o, 0), o1 = pk8(o, 8);
      o1.z = hi ? 0u : o1.z; o1.w = hi ? 0u : o1.w;
      const u32 so = WS_OB + ((t * 4 + k) * 16 + oh) * 2048;
      st16<0>(o0, l16, sWS, so); st16<1024>(o1, l16, sWS, so);
    }
    WAITV(0);
    flag(tagv, flane, sWS, WS_FO + (t * 16 + oh) * 8);
    STAMP(9);
  } else {
    const u32 tgG = ut * 2 + ch;
    const u32 fhC = WS_FH + tgG * 512;
    u32x2 pl;
#pragma unroll
    for (int s = 0; s < 28; ++s) {
      LAS char* ph = sm + L_R0 + s * 2048 + a128;
      accH1 = MFMA(tr2(ph, ph + 512), xf[s], accH1);
      if (s == 18) { SBAR(); ld8(pl, lane * 8, sWS, fhC); SBAR(); }
    }
    waitr<0>(pl);
    flag(tagv, flane, sWS, ch == 0 ? fhP + 128 : WS_FQ + ((t * 16 + hd) * 4 + tg) * 8);
    BAR();
    STAMP(5);
    u64 fm = spin(tagmask(pl, tagv), 0x3fffull, lane * 8, sWS, fhC, tagv);
    u32x4 A0[7], A1[7], A2[7], A3[7], A4[8];
    const u32 hbC = WS_HB + (tgG * 4 * 28 + 7 * tg) * 1024;
#define LOADA(A, so)                                                                                                    \
  do {                                                                                                                  \
    ld16<0>(A[0], l16, sWS, (so)); ld16<1024>(A[1], l16, sWS, (so)); ld16<2048>(A[2], l16, sWS, (so));                  \
    ld16<3072>(A[3], l16, sWS, (so)); ld16<0>(A[4], l16, sWS, (so) + 4096); ld16<1024>(A[5], l16, sWS, (so) + 4096);    \
    ld16<2048>(A[6], l16, sWS, (so) + 4096);                                                                            \
  } while (0)
    LOADA(A0, hbC);
    STAMP(6);
#pragma unroll
    for (int e = 0; e < 16; ++e) accH1[e] = gelu1(accH1[e], cg);
    {
      const u32 so = hbP + (2 + ch) * 28 * 1024;
      st16<0>(pk8(accH1, 0), l16, sWS, so);
      st16<1024>(pk8(accH1, 8), l16, sWS, so);
    }
    if (ch == 0) {
      BAR();
      ld8(pl, lane * 8, sWS, fhC);
#pragma unroll
      for (int n = 0; n < 14; ++n) dma(mb + L_R0 + (n >> 1) * 8192 + (n & 1) * 1024, (n & 1) ? vb + 8 * 896 : vb, sW2, rowoff[n >> 1]);
      waitr<14>(pl);
      flag(tagv, flane, sWS, fhP + 256);
      fm = tagmask(pl, tagv);
    }
    fm = spin(fm, 0x3fffull << 16, lane * 8, sWS, fhC, tagv);
    LOADA(A1, hbC + 28 * 1024);
    STAMP(7);
    f16v acc0 = {}, acc1 = {};
    const u32 ab0 = tg * 2048 + (4 * h + q) * 128 + ((q >> 1) << 6) + grp * 32 + p * 8, ab1 = ab0 ^ 64;
#define SEG(A, R, N0)                                                   \
  do {                                                                  \
    _Pragma("unroll") for (int u = 0; u < 7; ++u) {                     \
      LAS char* p0 = sm + (R) + u * 8192 + ab0;                         \
      LAS char* p1 = sm + (R) + u * 8192 + ab1;                         \
      u32x4 f0 = tr2(p0, p0 + 1024), f1 = tr2(p1, p1 + 1024);           \
      asm volatile("s_waitcnt vmcnt(%1)" : "+v"(A[u]) : "i"((N0) + 6 - u)); \
      acc0 = MFMA(f0, A[u], acc0);                                      \
      acc1 = MFMA(f1, A[u], acc1);                                      \
    }                                                                   \
  } while (0)
    const u32 vbh = vb + ch * 8 * 896, mbh = mb + ch * 1024;
#define STAGEB(R, so) _Pragma("unroll") for (int u = 0; u < 7; ++u) dma(mbh + (R) + u * 8192, vbh + (so) * 896, sW2, rowoff[u])
    SEG(A0, L_R1, 9);
    WAITV(7);
    if (ch == 1) flag(tagv, flane, sWS, fhP + 384);
    BAR();
    STAMP(8);
    ld8(pl, lane * 8, sWS, fhC);
    STAGEB(L_R1, 64);
    waitr<7>(pl);
    fm = spin(tagmask(pl, tagv), 0x3fffull << 32, lane * 8, sWS, fhC, tagv);
    LOADA(A2, hbC + 2 * 28 * 1024);
    SEG(A1, L_R0, 14);
    WAITV_BAR(7);
    STAMP(9);
    ld8(pl, lane * 8, sWS, fhC);
    STAGEB(L_R0, 96);
    SEG(A2, L_R1, 8);
    waitr<7>(pl);
    fm = spin(tagmask(pl, tagv), 0x3fffull << 48, lane * 8, sWS, fhC, tagv);
    LOADA(A3, hbC + 3 * 28 * 1024);
    WAITV_BAR(7);
    STAMP(10);
    const u32 foC = WS_FO + (ut >> 1) * 128;
    ld8(pl, (lane & 15) * 8, sWS, foC);
    {
      const u32x4 sWO = mksrd(wo, 448 * 896);
#pragma unroll
      for (int u = 0; u < 7; ++u) dma(lds0 + L_R1 + tg * 14336 + ch * 1024 + u * 2048, vbh, sWO, (tg * 112 + u * 16) * 896);
    }
    SEG(A3, L_R0, 8);
    waitr<7>(pl);
    spin(tagmask(pl, tagv), 0xffffull, (lane & 15) * 8, sWS, foC, tagv);
    STAMP(11);
    u32 omg;
    ld4(omg, r * 4, sWS, WS_OMG + tgG * 128);
    {
      const u32 so = WS_OB + (tgG * 16 + 4 * tg) * 2048;
      LOADA(A4, so);
      ld16<3072>(A4[7], l16, sWS, so + 4096);
    }
    WAITV_BAR(9);
    STAMP(12);
    {
      const u32 ao0 = ab0 + tg * (14336 - 2048), ao1 = ao0 ^ 64;
#pragma unroll
      for (int u = 0; u < 8; ++u) {
        LAS char* p0 = sm + L_R1 + (28 * (u >> 1) + 16 * (u & 1)) * 128 + ao0;
        LAS char* p1 = sm + L_R1 + (28 * (u >> 1) + 16 * (u & 1)) * 128 + ao1;
        u32x4 f0 = tr2(p0, p0 + 1024), f1 = tr2(p1, p1 + 1024);
        asm volatile("s_waitcnt vmcnt(%1)" : "+v"(A4[u]) : "i"(7 - u));
        acc0 = MFMA(f0, A4[u], acc0);
        acc1 = MFMA(f1, A4[u], acc1);
      }
    }
    asm volatile("" : "+v"(omg));
    STAMP(13);
    LAS char* red = sm + (ch ? L_RED1 : L_RED0) + l16;
#pragma unroll
    for (int g = 0; g < 4; ++g) {
      *(LAS f4*)(red + (g >> 1) * 8192 + tg * 2048 + (g & 1) * 1024) = (f4){acc0[4 * g], acc0[4 * g + 1], acc0[4 * g + 2], acc0[4 * g + 3]};
      *(LAS f4*)(red + (2 + (g >> 1)) * 8192 + tg * 2048 + (g & 1) * 1024) = (f4){acc1[4 * g], acc1[4 * g + 1], acc1[4 * g + 2], acc1[4 * g + 3]};
    }
    const u32x2 bA = *(LAS u32x2*)(sm + L_B2 + tg * 32 + h * 8), bB = *(LAS u32x2*)(sm + L_B2 + tg * 32 + 16 + h * 8);
    BAR();
    const float om = __uint_as_float(omg);
    f4 sa = (f4){blo(bA.x), bhi(bA.x), blo(bA.y), bhi(bA.y)} * om, sb = (f4){blo(bB.x), bhi(bB.x), blo(bB.y), bhi(bB.y)} * om;
#pragma unroll
    for (int k = 0; k < 4; ++k) {
      sa += *(LAS f4*)(red + tg * 8192 + k * 2048);
      sb += *(LAS f4*)(red + tg * 8192 + k * 2048 + 1024);
    }
    auto s0 = __builtin_amdgcn_permlane32_swap(pk(sa[0], sa[1]), pk(sb[0], sb[1]), false, false);
    auto s1 = __builtin_amdgcn_permlane32_swap(pk(sa[2], sa[3]), pk(sb[2], sb[3]), false, false);
    *(u32x4*)((char*)out + (tgG * 32 + r) * 896 + ct * 128 + tg * 32 + h * 16) = (u32x4){s0[0], s1[0], s0[1], s1[1]};
    if (b == 0 && w == 0) flag(tagv, flane, sWS, WS_EPOCH);
    STAMP(14);
  }
  WAITV(0);
  STAMP(15);
  if (b == 0 && tid == 0) { ((volatile u32*)(ws + WS_DBG))[1] = st[0]; ((volatile u32*)(ws + WS_DBG))[2] = st[15]; }
}

__global__ void pre_kernel(char* ws) { if (threadIdx.x == 0) ((volatile u32*)(ws + WS_DBG))[0] = (u32)__builtin_amdgcn_s_memrealtime(); }
__global__ void post_kernel(char* ws) { u32 t3 = (u32)__builtin_amdgcn_s_memrealtime(); volatile u32* d = (volatile u32*)(ws + WS_DBG); u32 t0 = d[0], t1 = d[1], t2 = d[2]; u32 dt = 10u * (0*(t0+t1+t2)); if (dt > 100000u) dt = 100000u;
  while ((u32)__builtin_amdgcn_s_memrealtime() - t3 < dt) __builtin_amdgcn_s_sleep(1); }
extern "C" void kernel_launch(void* const* d_in, const int* in_sizes, int n_in, void* d_out, int out_size, void* d_ws, size_t ws_size,
                              hipStream_t stream) {
  pre_kernel<<<1, 64, 0, stream>>>((char*)d_ws);
  fused_kernel<<<256, 512, 0, stream>>>((const u16*)d_in[0], (const u16*)d_in[1], (const u16*)d_in[2], (const u16*)d_in[3], (const u16*)d_in[5],
                                        (const u16*)d_in[6], (const u16*)d_in[7], (const u16*)d_in[8], (const u16*)d_in[9], (u16*)d_out,
                                        (char*)d_ws);
  post_kernel<<<1, 64, 0, stream>>>((char*)d_ws);
}
```

```cpp
#include <hip/hip_runtime.h>
#include <stdint.h>

constexpr unsigned WS_BYTES = 0x1600000;
constexpr unsigned WS_DBG = 0x1500000;

#define BUF(n) const void* p##n, unsigned b##n, unsigned f##n
__global__ __launch_bounds__(512, 2) void fused_kernel(BUF(0), BUF(1), BUF(2), BUF(3), BUF(4), BUF(5), BUF(6), BUF(7), BUF(8), BUF(9), BUF(10)) {
  __shared__ __attribute__((aligned(16))) char smem[163840];
  unsigned tid = threadIdx.x;
  asm volatile(
    "s_load_dwordx16 s[4:19], s[0:1], 0\n"
    "s_load_dwordx16 s[20:35], s[0:1], 64\n"
    "s_load_dwordx8 s[36:43], s[0:1], 128\n"
    "s_load_dwordx4 s[44:47], s[0:1], 160\n"
    "v_lshrrev_b32 v26, 6, v0\n"
    "v_and_b32 v30, 63, v0\n"
    "v_readfirstlane_b32 s56, v26\n"
    "s_and_b32 s70, s2, 7\n"
    "s_bfe_u32 s71, s2, 0x20003\n"
    "s_lshl2_add_u32 s62, s70, s71\n"
    "s_and_b32 s59, s62, 15\n"
    "s_lshr_b32 s90, s62, 4\n"
    "s_lshr_b32 s61, s2, 5\n"
    "s_lshl3_add_u32 s60, s90, s61\n"
    "v_lshlrev_b32 v0, 4, v30\n"
    "v_lshrrev_b32 v26, 3, v30\n"
    "v_and_b32 v27, 7, v30\n"
    "v_lshrrev_b32 v29, 4, v30\n"
    "v_xor_b32 v29, v29, v27\n"
    "v_mul_u32_u24 v5, 896, v26\n"
    "v_lshl_add_u32 v1, v29, 4, v5\n"
    "v_xor_b32 v2, 64, v1\n"
    "v_add_u32 v2, 7168, v2\n"
    "v_lshrrev_b32 v29, 2, v30\n"
    "v_and_b32 v26, 4, v29\n"
    "v_xor_b32 v26, v26, v27\n"
    "v_lshlrev_b32 v26, 4, v26\n"
    "v_lshl_add_u32 v3, v5, 2, v26\n"
    "v_add_u32 v5, v5, v26\n"
    "s_lshl3_add_u32 s67, s59, s56\n"
    "s_mul_i32 s67, s67, 14336\n"
    "s_lshl_b32 s66, s56, 11\n"
    "s_lshl_b32 s65, s56, 10\n"
    "s_lshr_b32 s57, s56, 2\n"
    "s_and_b32 s58, s56, 3\n"
    "s_sub_u32 s91, s60, s90\n"
    "s_min_u32 s91, s91, 13\n"
    "s_lshl_b32 s91, s91, 8\n"
    "s_mul_i32 s68, s56, 28672\n"
    "s_add_u32 s68, s68, s91\n"
    "s_min_u32 s63, s61, 6\n"
    "s_lshl_b32 s63, s63, 7\n"
    "v_and_b32 v27, 3, v30\n"
    "v_lshlrev_b32 v4, 4, v27\n"
    "v_min_u32 v4, 40, v4\n"
    "v_mul_u32_u24 v26, 2688, v29\n"
    "v_add_u32 v4, v4, v26\n"
    "v_lshlrev_b32 v140, 1, v29\n"
    "v_lshlrev_b32 v143, 7, v30\n"
    "v_bfrev_b32 v28, 1\n"
    "s_and_b32 s70, s60, 1\n"
    "s_lshl1_add_u32 s70, s59, s70\n"
    "s_lshr_b32 s71, s56, 1\n"
    "s_mul_i32 s49, s70, 42\n"
    "s_add_u32 s49, s49, s71\n"
    "s_and_b32 s94, s56, 1\n"
    "s_and_b32 s48, s60, 14\n"
    "s_or_b32 s94, s94, s48\n"
    "s_lshl4_add_u32 s49, s49, s94\n"
    "s_mul_i32 s49, s49, 56\n"
    "s_mul_i32 s92, s60, 86016\n"
    "s_add_u32 s92, s92, 3145728\n"
    "s_mul_i32 s94, s94, 3\n"
    "s_add_u32 s71, s71, s94\n"
    "s_lshl_b32 s71, s71, 5\n"
    "s_add_u32 s71, s71, s70\n"
    "s_mul_i32 s71, s71, 896\n"
    "s_add_u32 s48, s71, 3145728\n"
    "s_mul_i32 s50, s70, 28\n"
    "s_mul_i32 s70, s2, 7168\n"
    "s_mul_i32 s69, s2, 6272\n"
    "s_cmp_eq_u32 s56, 6\n"
    "s_cselect_b32 s49, s70, s49\n"
    "s_cmp_eq_u32 s56, 7\n"
    "s_cselect_b32 s49, s69, s49\n"
    "s_cselect_b32 s70, 49, 56\n"
    "s_cmp_lt_u32 s56, 6\n"
    "s_cselect_b64 s[54:55], -1, 0\n"
    "v_cmp_gt_u32 vcc, s70, v30\n"
    "v_cndmask_b32 v4, v143, v4, s[54:55]\n"
    "s_and_b32 s55, s55, 0xffffff\n"
    "v_cndmask_b32 v4, v28, v4, vcc\n"
    "v_cmp_eq_u32 vcc, 3, v27\n"
    "s_cmp_lt_u32 s56, 2\n"
    "s_cselect_b64 s[98:99], vcc, 0\n"
    "v_and_b32 v22, 31, v30\n"
    "v_lshrrev_b32 v23, 5, v30\n"
    "v_and_b32 v24, 3, v29\n"
    "v_and_b32 v26, 4, v29\n"
    "v_or_b32 v25, v26, v27\n"
    "s_lshl_b32 s70, s58, 5\n"
    "v_add_u32 v26, s70, v22\n"
    "v_bfe_u32 v27, v30, 1, 3\n"
    "v_xor_b32 v27, v27, v23\n"
    "v_lshl_add_u32 v26, v26, 3, v27\n"
    "v_lshlrev_b32 v14, 3, v26\n"
    "v_add_u32 v14, 57344, v14\n"
    "v_lshlrev_b32 v26, 4, v26\n"
    "v_add_u32 v8, 32768, v26\n"
    "v_xor_b32 v9, 32, v8\n"
    "v_xor_b32 v10, 64, v8\n"
    "v_xor_b32 v11, 0x60, v8\n"
    "v_lshl_add_u32 v26, v23, 3, v24\n"
    "v_lshrrev_b32 v29, 1, v24\n"
    "v_xor_b32 v27, s57, v29\n"
    "v_lshl_add_u32 v27, v26, 1, v27\n"
    "v_lshl_add_u32 v27, v27, 3, v25\n"
    "v_lshlrev_b32 v27, 3, v27\n"
    "v_add_u32 v12, 32768, v27\n"
    "s_waitcnt lgkmcnt(0)\n"
    "s_cmp_eq_u32 s56, 6\n"
    "s_cselect_b64 s[12:13], s[4:5], s[12:13]\n"
    "s_cselect_b32 s14, s6, s14\n"
    "s_cmp_eq_u32 s56, 7\n"
    "s_cselect_b64 s[12:13], s[8:9], s[12:13]\n"
    "s_cselect_b32 s14, s10, s14\n"
    "buffer_load_dwordx4 v[128:131], v4, s[12:15], s49 offen\n"
    "buffer_load_ushort v132, v140, s[36:39], s50 offen\n"
    "s_cmp_eq_u32 s61, 7\n"
    "s_cselect_b32 s64, 1, 0\n"
    "s_cselect_b32 s10, 0, s10\n"
    "s_add_u32 m0, s66, 32768\n"
    "s_nop 0\n"
    "buffer_load_dwordx4 v1, s[4:7], s67 offen lds\n"
    "s_add_u32 m0, s66, 33792\n"
    "s_nop 0\n"
    "buffer_load_dwordx4 v2, s[4:7], s67 offen lds\n"
    "s_add_u32 m0, s65, 49152\n"
    "s_add_u32 s94, s68, 0\n"
    "buffer_load_dwordx4 v3, s[8:11], s94 offen lds\n"
    "s_mul_i32 s94, s60, 1568\n"
    "s_add_u32 s94, s94, s65\n"
    "s_add_u32 s86, s65, 159744\n"
    "s_cmp_eq_u32 s56, 3\n"
    "s_cselect_b64 s[32:33], s[40:41], s[32:33]\n"
    "s_cselect_b32 s34, s42, s34\n"
    "s_cselect_b32 s94, s91, s94\n"
    "s_cselect_b64 exec, 0xffff, -1\n"
    "s_cmp_eq_u32 s56, 4\n"
    "s_cselect_b64 s[32:33], s[44:45], s[32:33]\n"
    "s_cselect_b32 s34, s46, s34\n"
    "s_cselect_b32 s94, s63, s94\n"
    "s_cselect_b32 s86, 163072, s86\n"
    "s_cselect_b64 exec, 0xff, exec\n"
    "s_bitcmp1_b32 0xe4, s56\n"
    "s_cselect_b64 s[32:33], s[16:17], s[32:33]\n"
    "s_cselect_b32 s94, 0, s94\n"
    "s_cselect_b32 m0, 163328, s86\n"
    "s_cselect_b64 exec, 1, exec\n"
    "buffer_load_dwordx4 v0, s[32:35], s94 offen sc1 lds\n"
    "s_mov_b64 exec, -1\n"
    "s_and_b32 s94, s2, 0xf8\n"
    "s_add_u32 s94, s94, s56\n"
    "s_lshl_b32 s94, s94, 7\n"
    "s_getpc_b64 s[0:1]\n"
    ".Lpc_%=:\n"
    "s_min_u32 s94, s94, .Llast_%=-.Lpc_%=-128\n"
    "s_add_u32 s0, s0, s94\n"
    "s_addc_u32 s1, s1, 0\n"
    "s_load_dword s3, s[0:1], 0\n"
    "s_load_dword s3, s[0:1], 64\n"
    "v_cmp_lt_u32 vcc, 5, v25\n"
    "s_lshl_b32 s71, s57, 6\n"
    "s_add_u32 s71, s71, 162816\n"
    "v_cndmask_b32 v27, 0, 1, vcc\n"
    "v_xor_b32 v27, v27, v25\n"
    "v_lshl_add_u32 v27, v26, 3, v27\n"
    "v_lshl_add_u32 v20, v23, 3, s71\n"
    "s_mul_i32 s71, s57, 28672\n"
    "s_add_u32 s71, s71, 57344\n"
    "v_lshl_add_u32 v13, v27, 3, s71\n"
    "v_lshl_add_u32 v27, v23, 2, v29\n"
    "v_xor_b32 v27, v27, v25\n"
    "v_lshl_add_u32 v26, v26, 3, v27\n"
    "v_lshlrev_b32 v133, 3, v26\n"
    "v_lshl_add_u32 v26, v23, 2, v24\n"
    "v_min_u32 v27, 6, v25\n"
    "v_mad_u32_u24 v27, v26, 7, v27\n"
    "v_lshlrev_b32 v27, 3, v27\n"
    "v_add_u32 v15, 159744, v27\n"
    "v_lshl_add_u32 v26, v26, 1, v29\n"
    "v_lshl_add_u32 v26, v26, 3, v25\n"
    "s_lshl_b32 s85, s58, 11\n"
    "v_lshl_add_u32 v26, v26, 3, s85\n"
    "v_xor_b32 v141, s70, v26\n"
    "v_cmp_gt_u32 vcc, 8, v30\n"
    "s_lshl_b32 s93, s58, 10\n"
    "s_add_u32 s92, s92, s93\n"
    "v_cndmask_b32 v7, v28, v0, vcc\n"
    "s_cmp_eq_u32 s58, 3\n"
    "s_cselect_b32 s70, 0, 0x80000000\n"
    "v_or_b32 v142, s70, v7\n"
    "v_mov_b32 v21, 0xc0135761\n"
    "s_mov_b32 s88, 0xbdd2d3e8\n"
    "s_mov_b32 s87, 0x3fb8aa3b\n"
    "v_lshl_add_u32 v26, v30, 2, s58\n"
    "v_cmp_gt_u32 vcc, 14, v26\n"
    "v_add_u32 v27, -14, v26\n"
    "v_min_u32 v26, v26, v27\n"
    "v_cndmask_b32 v29, 0, 2, vcc\n"
    "v_and_b32 v27, 1, v26\n"
    "v_and_b32 v26, -2, v26\n"
    "v_lshl_add_u32 v26, v26, 2, v27\n"
    "s_and_b32 s70, s62, 1\n"
    "s_mul_i32 s71, s70, 56\n"
    "v_add3_u32 v26, v26, v29, s71\n"
    "v_mul_u32_u24 v26, 14336, v26\n"
    "v_add_u32 v26, s63, v26\n"
    "v_mov_b32 v27, 163328\n"
    "v_readlane_b32 s72, v26, 0\n"
    "v_readlane_b32 s73, v26, 1\n"
    "v_readlane_b32 s74, v26, 2\n"
    "v_readlane_b32 s75, v26, 3\n"
    "v_readlane_b32 s76, v26, 4\n"
    "v_readlane_b32 s77, v26, 5\n"
    "v_readlane_b32 s78, v26, 6\n"
    "s_lshl4_add_u32 s51, s60, s59\n"
    "s_lshl_b32 s51, s51, 7\n"
    "s_add_u32 s51, s51, 32768\n"
    "s_lshl2_add_u32 s70, s59, s58\n"
    "s_mul_i32 s70, s70, 208896\n"
    "s_add_u32 s70, s70, 5242880\n"
    "s_mul_i32 s71, s90, 28\n"
    "s_add_u32 s71, s71, s61\n"
    "s_lshl_b32 s71, s71, 11\n"
    "s_add_u32 s79, s70, s71\n"
    "s_lshl_b32 s71, s60, 7\n"
    "s_lshl4_add_u32 s81, s71, s70\n"
    "s_add_u32 s81, s81, 114688\n"
    "s_lshl_b32 s82, s90, 7\n"
    "s_add_u32 s82, s82, s70\n"
    "s_add_u32 s82, s82, 147456\n"
    "s_lshl_b32 s70, s59, 13\n"
    "s_add_u32 s80, s70, s71\n"
    "s_add_u32 s80, s80, 131072\n"
    "s_lshl1_add_u32 s83, s60, s58\n"
    "s_lshl4_add_u32 s83, s83, s59\n"
    "s_lshl_b32 s84, s83, 7\n"
    "s_add_u32 s84, s84, 65536\n"
    "s_lshl_b32 s83, s83, 12\n"
    "s_add_u32 s83, s83, 1048576\n"
    "v_cndmask_b32 v4, v28, v0, s[54:55]\n"
    "v_cmp_gt_u32 vcc, 7, v30\n"
    "s_mov_b32 s54, 0\n"
    "s_mov_b32 s55, -1\n"
    "v_cndmask_b32 v143, v28, v143, vcc\n"
    "v_add_u32 v140, 57344, v133\n"
    "s_lshl1_add_u32 s70, s62, s57\n"
    "s_mul_i32 s46, s70, 28672\n"
    "s_mul_i32 s70, s70, 208896\n"
    "s_add_u32 s70, s70, 5242880\n"
    "s_and_b32 s90, s62, 1\n"
    "s_mul_i32 s91, s90, 4294131712\n"
    "s_add_u32 s91, s91, 417792\n"
    "s_mul_i32 s71, s90, 57344\n"
    "s_add_u32 s71, s71, s93\n"
    "s_add_u32 s36, s70, s71\n"
    "s_add_u32 s37, s36, s91\n"
    "s_lshl3_add_u32 s39, s93, s70\n"
    "s_add_u32 s39, s39, 114688\n"
    "s_lshl_b32 s71, s90, 7\n"
    "s_add_u32 s40, s70, s71\n"
    "s_add_u32 s40, s40, 147456\n"
    "s_lshl_b32 s71, s63, 6\n"
    "s_add_u32 s44, s70, s71\n"
    "s_add_u32 s44, s44, 151552\n"
    "s_add_u32 s43, s44, s91\n"
    "s_add_u32 s43, s43, s85\n"
    "s_lshr_b32 s71, s62, 1\n"
    "s_lshl3_add_u32 s71, s71, s90\n"
    "s_lshl_b32 s71, s71, 10\n"
    "s_add_u32 s38, s71, 131072\n"
    "s_lshl_b32 s42, s60, 13\n"
    "s_add_u32 s42, s42, 262144\n"
    "s_add_u32 s41, s42, s85\n"
    "s_mul_i32 s45, s58, 7168\n"
    "s_add_u32 s45, s45, s63\n"
    "s_add_u32 s46, s46, s45\n"
    "s_add_u32 m0, s66, 130944\n"
    "s_nop 0\n"
    "buffer_load_dwordx4 v1, s[4:7], s67 offen offset:128 lds\n"
    "s_waitcnt vmcnt(5)\n"
    "v_cndmask_b32 v128, v128, v132, s[98:99]\n"
    "buffer_store_dwordx4 v[128:131], v4, s[16:19], s48 offen offset:0 sc1\n"
    "s_add_u32 m0, s66, 131968\n"
    "s_nop 0\n"
    "buffer_load_dwordx4 v2, s[4:7], s67 offen offset:128 lds\n"
    "s_add_u32 m0, s65, 147456\n"
    "s_add_u32 s94, s68, 229376\n"
    "buffer_load_dwordx4 v3, s[8:11], s94 offen lds\n"
    "s_barrier\n"
    "s_add_u32 m0, s66, 57088\n"
    "s_nop 0\n"
    "buffer_load_dwordx4 v1, s[4:7], s67 offen offset:256 lds\n"
    "s_add_u32 m0, s66, 58112\n"
    "s_nop 0\n"
    "buffer_load_dwordx4 v2, s[4:7], s67 offen offset:256 lds\n"
    "s_add_u32 m0, s65, 73728\n"
    "s_add_u32 s94, s68, 458752\n"
    "buffer_load_dwordx4 v3, s[8:11], s94 offen lds\n"
    "s_lshl_b32 s70, s64, 31\n"
    "v_or_b32 v4, s70, v0\n"
    "v_or_b32 v142, s70, v142\n"
    "s_waitcnt vmcnt(7)\n"
    "s_barrier\n"
    "ds_read_b64 v[16:17], v27\n"
    "ds_read_b64 v[112:113], v20 offset:0\n"
    "ds_read_b64 v[114:115], v20 offset:16\n"
    "ds_read_b64 v[116:117], v20 offset:32\n"
    "ds_read_b64 v[118:119], v20 offset:48\n"
    "s_add_u32 m0, s66, 81536\n"
    "s_nop 0\n"
    "buffer_load_dwordx4 v1, s[4:7], s67 offen offset:384 lds\n"
    "ds_read_b128 v[144:147], v8 offset:0\n"
    "ds_read_b64_tr_b16 v[80:81], v12 offset:16384\n"
    "ds_read_b64_tr_b16 v[82:83], v12 offset:16896\n"
    "s_waitcnt lgkmcnt(3)\n"
    "ds_read_b64 v[120:121], v20 offset:128\n"
    "ds_read_b64 v[122:123], v20 offset:144\n"
    "ds_read_b64 v[124:125], v20 offset:160\n"
    "ds_read_b64 v[126:127], v20 offset:176\n"
    "v_readfirstlane_b32 s52, v16\n"
    "v_readfirstlane_b32 s53, v17\n"
    "s_add_u32 s52, s52, 0x7F4A7C15\n"
    "s_addc_u32 s53, s53, 0x9E3779B9\n"
    "v_mov_b32 v16, s52\n"
    "v_mov_b32 v17, s53\n"
    "v_mov_b32 v18, s52\n"
    "v_mov_b32 v19, s53\n"
    "v_lshlrev_b32 v32, 16, v112\n"
    "v_and_b32 v33, 0xffff0000, v112\n"
    "v_lshlrev_b32 v34, 16, v113\n"
    "v_and_b32 v35, 0xffff0000, v113\n"
    "s_add_u32 m0, s66, 82560\n"
    "s_nop 0\n"
    "buffer_load_dwordx4 v2, s[4:7], s67 offen offset:384 lds\n"
    "ds_read_b128 v[148:151], v9 offset:0\n"
    "ds_read_b64_tr_b16 v[84:85], v12 offset:18432\n"
    "ds_read_b64_tr_b16 v[86:87], v12 offset:18944\n"
    "s_and_b32 s12, s52, 0xff\n"
    "s_bfe_u32 s13, s52, 0x80008\n"
    "s_bfe_u32 s14, s52, 0x80010\n"
    "s_lshr_b32 s15, s52, 24\n"
    "s_lshl_b32 s47, s52, 16\n"
    "s_lshl_b32 s70, s57, 12\n"
    "s_add_u32 s49, s92, s70\n"
    "v_lshlrev_b32 v36, 16, v114\n"
    "v_and_b32 v37, 0xffff0000, v114\n"
    "v_lshlrev_b32 v38, 16, v115\n"
    "v_and_b32 v39, 0xffff0000, v115\n"
    "s_add_u32 m0, s65, 98304\n"
    "s_add_u32 s94, s68, 688128\n"
    "buffer_load_dwordx4 v3, s[8:11], s94 offen lds\n"
    "ds_read_b128 v[152:155], v10 offset:0\n"
    "ds_read_b64_tr_b16 v[88:89], v12 offset:20480\n"
    "ds_read_b64_tr_b16 v[90:91], v12 offset:20992\n"
    "v_lshlrev_b32 v40, 16, v116\n"
    "v_and_b32 v41, 0xffff0000, v116\n"
    "v_lshlrev_b32 v42, 16, v117\n"
    "v_and_b32 v43, 0xffff0000, v117\n"
    "ds_read_b128 v[156:159], v11 offset:0\n"
    "ds_read_b64_tr_b16 v[92:93], v12 offset:22528\n"
    "ds_read_b64_tr_b16 v[94:95], v12 offset:23040\n"
    "v_lshlrev_b32 v44, 16, v118\n"
    "v_and_b32 v45, 0xffff0000, v118\n"
    "v_lshlrev_b32 v46, 16, v119\n"
    "v_and_b32 v47, 0xffff0000, v119\n"
    "v_add_u32 v8, 98304, v8\n"
    "v_add_u32 v9, 98304, v9\n"
    "v_add_u32 v10, 98304, v10\n"
    "v_add_u32 v11, 98304, v11\n"
    "v_add_u32 v12, 98304, v12\n"
    "s_waitcnt vmcnt(6)\n"
    "s_barrier\n"
    "s_cmp_eq_u32 s56, 0\n"
    "s_cbranch_scc0 .Lw1_%=\n"
    "buffer_store_dwordx4 v[16:19], v7, s[16:19], s51 offen offset:0 sc1\n"
    ".Lw1_%=:\n"
    "s_add_u32 m0, s66, 105984\n"
    "s_nop 0\n"
    "buffer_load_dwordx4 v1, s[4:7], s67 offen offset:512 lds\n"
    "s_sleep 2\n"
    "ds_read_b128 v[160:163], v8 offset:0\n"
    "ds_read_b64_tr_b16 v[96:97], v12 offset:16384\n"
    "ds_read_b64_tr_b16 v[98:99], v12 offset:16896\n"
    "s_waitcnt lgkmcnt(12)\n"
    "v_mfma_f32_32x32x16_bf16 v[32:47], v[80:83], v[144:147], v[32:47]\n"
    "v_lshlrev_b32 v64, 16, v120\n"
    "v_and_b32 v65, 0xffff0000, v120\n"
    "v_lshlrev_b32 v66, 16, v121\n"
    "v_and_b32 v67, 0xffff0000, v121\n"
    "s_add_u32 m0, s66, 107008\n"
    "s_nop 0\n"
    "buffer_load_dwordx4 v2, s[4:7], s67 offen offset:512 lds\n"
    "ds_read_b128 v[164:167], v9 offset:0\n"
    "ds_read_b64_tr_b16 v[100:101], v12 offset:18432\n"
    "ds_read_b64_tr_b16 v[102:103], v12 offset:18944\n"
    "s_waitcnt lgkmcnt(12)\n"
    "v_mfma_f32_32x32x16_bf16 v[32:47], v[84:87], v[148:151], v[32:47]\n"
    "v_lshlrev_b32 v68, 16, v122\n"
    "v_and_b32 v69, 0xffff0000, v122\n"
    "v_lshlrev_b32 v70, 16, v123\n"
    "v_and_b32 v71, 0xffff0000, v123\n"
    "s_add_u32 m0, s65, 122880\n"
    "s_add_u32 s94, s68, 917504\n"
    "buffer_load_dwordx4 v3, s[8:11], s94 offen lds\n"
    "ds_read_b128 v[168:171], v10 offset:0\n"
    "ds_read_b64_tr_b16 v[104:105], v12 offset:20480\n"
    "ds_read_b64_tr_b16 v[106:107], v12 offset:20992\n"
    "s_waitcnt lgkmcnt(12)\n"
    "v_mfma_f32_32x32x16_bf16 v[32:47], v[88:91], v[152:155], v[32:47]\n"
    "v_lshlrev_b32 v72, 16, v124\n"
    "v_and_b32 v73, 0xffff0000, v124\n"
    "v_lshlrev_b32 v74, 16, v125\n"
    "v_and_b32 v75, 0xffff0000, v125\n"
    "ds_read_b128 v[172:175], v11 offset:0\n"
    "ds_read_b64_tr_b16 v[108:109], v12 offset:22528\n"
    "ds_read_b64_tr_b16 v[110:111], v12 offset:23040\n"
    "s_waitcnt lgkmcnt(12)\n"
    "v_mfma_f32_32x32x16_bf16 v[32:47], v[92:95], v[156:159], v[32:47]\n"
    "v_lshlrev_b32 v76, 16, v126\n"
    "v_and_b32 v77, 0xffff0000, v126\n"
    "v_lshlrev_b32 v78, 16, v127\n"
    "v_and_b32 v79, 0xffff0000, v127\n"
    "v_add_u32 v8, 4294893568, v8\n"
    "v_add_u32 v9, 4294893568, v9\n"
    "v_add_u32 v10, 4294893568, v10\n"
    "v_add_u32 v11, 4294893568, v11\n"
    "v_add_u32 v12, 4294893568, v12\n"
    "s_waitcnt vmcnt(6)\n"
    "s_barrier\n"
    "s_add_u32 m0, s66, 32128\n"
    "s_nop 0\n"
    "buffer_load_dwordx4 v1, s[4:7], s67 offen offset:640 lds\n"
    "s_sleep 3\n"
    "ds_read_b128 v[176:179], v8 offset:0\n"
    "ds_read_b64_tr_b16 v[80:81], v12 offset:16384\n"
    "ds_read_b64_tr_b16 v[82:83], v12 offset:16896\n"
    "s_waitcnt lgkmcnt(12)\n"
    "v_mfma_f32_32x32x16_bf16 v[32:47], v[96:99], v[160:163], v[32:47]\n"
    "s_add_u32 m0, s66, 33152\n"
    "s_nop 0\n"
    "buffer_load_dwordx4 v2, s[4:7], s67 offen offset:640 lds\n"
    "ds_read_b128 v[180:183], v9 offset:0\n"
    "ds_read_b64_tr_b16 v[84:85], v12 offset:18432\n"
    "ds_read_b64_tr_b16 v[86:87], v12 offset:18944\n"
    "s_waitcnt lgkmcnt(12)\n"
    "v_mfma_f32_32x32x16_bf16 v[32:47], v[100:103], v[164:167], v[32:47]\n"
    "s_add_u32 m0, s65, 49152\n"
    "s_add_u32 s94, s68, 1146880\n"
    "buffer_load_dwordx4 v3, s[8:11], s94 offen lds\n"
    "ds_read_b128 v[184:187], v10 offset:0\n"
    "ds_read_b64_tr_b16 v[88:89], v12 offset:20480\n"
    "ds_read_b64_tr_b16 v[90:91], v12 offset:20992\n"
    "s_waitcnt lgkmcnt(12)\n"
    "v_mfma_f32_32x32x16_bf16 v[32:47], v[104:107], v[168:171], v[32:47]\n"
    "ds_read_b128 v[188:191], v11 offset:0\n"
    "ds_read_b64_tr_b16 v[92:93], v12 offset:22528\n"
    "ds_read_b64_tr_b16 v[94:95], v12 offset:23040\n"
    "s_waitcnt lgkmcnt(12)\n"
    "v_mfma_f32_32x32x16_bf16 v[32:47], v[108:111], v[172:175], v[32:47]\n"
    "s_waitcnt vmcnt(6)\n"
    "s_barrier\n"
    "s_add_u32 m0, s66, 130304\n"
    "s_nop 0\n"
    "buffer_load_dwordx4 v1, s[4:7], s67 offen offset:768 lds\n"
    "s_sleep 3\n"
    "ds_read_b128 v[192:195], v8 offset:24576\n"
    "ds_read_b64_tr_b16 v[96:97], v12 offset:40960\n"
    "ds_read_b64_tr_b16 v[98:99], v12 offset:41472\n"
    "s_waitcnt lgkmcnt(12)\n"
    "v_mfma_f32_32x32x16_bf16 v[32:47], v[80:83], v[176:179], v[32:47]\n"
    "s_add_u32 m0, s66, 131328\n"
    "s_nop 0\n"
    "buffer_load_dwordx4 v2, s[4:7], s67 offen offset:768 lds\n"
    "ds_read_b128 v[196:199], v9 offset:24576\n"
    "ds_read_b64_tr_b16 v[100:101], v12 offset:43008\n"
    "ds_read_b64_tr_b16 v[102:103], v12 offset:43520\n"
    "s_waitcnt lgkmcnt(12)\n"
    "v_mfma_f32_32x32x16_bf16 v[32:47], v[84:87], v[180:183], v[32:47]\n"
    "s_add_u32 m0, s65, 147456\n"
    "s_add_u32 s94, s68, 1376256\n"
    "buffer_load_dwordx4 v3, s[8:11], s94 offen lds\n"
    "ds_read_b128 v[200:203], v10 offset:24576\n"
    "ds_read_b64_tr_b16 v[104:105], v12 offset:45056\n"
    "ds_read_b64_tr_b16 v[106:107], v12 offset:45568\n"
    "s_waitcnt lgkmcnt(12)\n"
    "v_mfma_f32_32x32x16_bf16 v[32:47], v[88:91], v[184:187], v[32:47]\n"
    "s_cmp_eq_u32 s56, 3\n"
    "s_cbranch_scc0 .Lw2_%=\n"
    "s_and_b32 s51, s51, 0xfffff000\n"
    "v_lshlrev_b32 v29, 7, v30\n"
    "v_cmp_gt_u32 vcc, 32, v30\n"
    "s_nop 1\n"
    "v_cndmask_b32 v20, v28, v29, vcc\n"
    "buffer_load_dwordx2 v[26:27], v20, s[16:19], s51 offen offset:0 sc1\n"
    ".Lw2_%=:\n"
    "ds_read_b128 v[204:207], v11 offset:24576\n"
    "ds_read_b64_tr_b16 v[108:109], v12 offset:47104\n"
    "ds_read_b64_tr_b16 v[110:111], v12 offset:47616\n"
    "s_waitcnt lgkmcnt(12)\n"
    "v_mfma_f32_32x32x16_bf16 v[32:47], v[92:95], v[188:191], v[32:47]\n"
    "v_add_u32 v8, 49152, v8\n"
    "v_add_u32 v9, 49152, v9\n"
    "v_add_u32 v10, 49152, v10\n"
    "v_add_u32 v11, 49152, v11\n"
    "v_add_u32 v12, 49152, v12\n"
    "s_waitcnt vmcnt(6)\n"
    "s_barrier\n"
    "s_sleep 3\n"
    "s_add_u32 m0, s65, 0\n"
    "s_add_u32 s94, s68, 128\n"
    "buffer_load_dwordx4 v3, s[8:11], s94 offen lds\n"
    "ds_read_b128 v[208:211], v8 offset:0\n"
    "ds_read_b64_tr_b16 v[80:81], v12 offset:16384\n"
    "ds_read_b64_tr_b16 v[82:83], v12 offset:16896\n"
    "s_waitcnt lgkmcnt(12)\n"
    "v_mfma_f32_32x32x16_bf16 v[32:47], v[96:99], v[192:195], v[32:47]\n"
    "s_add_u32 m0, s65, 8192\n"
    "s_add_u32 s94, s68, 229504\n"
    "buffer_load_dwordx4 v3, s[8:11], s94 offen lds\n"
    "ds_read_b128 v[212:215], v9 offset:0\n"
    "ds_read_b64_tr_b16 v[84:85], v12 offset:18432\n"
    "ds_read_b64_tr_b16 v[86:87], v12 offset:18944\n"
    "s_waitcnt lgkmcnt(12)\n"
    "v_mfma_f32_32x32x16_bf16 v[32:47], v[100:103], v[196:199], v[32:47]\n"
    "s_add_u32 m0, s65, 16384\n"
    "s_add_u32 s94, s68, 458880\n"
    "buffer_load_dwordx4 v3, s[8:11], s94 offen lds\n"
    "ds_read_b128 v[216:219], v10 offset:0\n"
    "ds_read_b64_tr_b16 v[88:89], v12 offset:20480\n"
    "ds_read_b64_tr_b16 v[90:91], v12 offset:20992\n"
    "s_waitcnt lgkmcnt(12)\n"
    "v_mfma_f32_32x32x16_bf16 v[32:47], v[104:107], v[200:203], v[32:47]\n"
    "ds_read_b128 v[220:223], v11 offset:0\n"
    "ds_read_b64_tr_b16 v[92:93], v12 offset:22528\n"
    "ds_read_b64_tr_b16 v[94:95], v12 offset:23040\n"
    "s_waitcnt lgkmcnt(12)\n"
    "v_mfma_f32_32x32x16_bf16 v[32:47], v[108:111], v[204:207], v[32:47]\n"
    "v_add_u32 v8, 4294893568, v8\n"
    "v_add_u32 v9, 4294893568, v9\n"
    "v_add_u32 v10, 4294893568, v10\n"
    "v_add_u32 v11, 4294893568, v11\n"
    "v_add_u32 v12, 4294893568, v12\n"
    "s_cmp_eq_u32 s56, 3\n"
    "s_cbranch_scc0 .Lw3_%=\n"
    "s_waitcnt vmcnt(3)\n"
    "v_cmp_eq_u64 vcc, s[52:53], v[26:27]\n"
    "s_nop 0\n"
    "s_andn2_b32 s70, -1, vcc_lo\n"
    "s_cbranch_scc1 .Lspin0_%=\n"
    ".Lok0_%=:\n"
    ".Lw3_%=:\n"
    "s_waitcnt vmcnt(6)\n"
    "s_barrier\n"
    "s_sleep 2\n"
    "s_add_u32 m0, s65, 57344\n"
    "s_add_u32 s94, s49, 28672\n"
    "buffer_load_dwordx4 v0, s[16:19], s94 offen sc1 lds\n"
    "s_add_u32 m0, s65, 65536\n"
    "s_add_u32 s94, s49, 36864\n"
    "buffer_load_dwordx4 v0, s[16:19], s94 offen sc1 lds\n"
    "ds_read_b128 v[224:227], v8 offset:0\n"
    "ds_read_b64_tr_b16 v[96:97], v12 offset:16384\n"
    "ds_read_b64_tr_b16 v[98:99], v12 offset:16896\n"
    "s_waitcnt lgkmcnt(12)\n"
    "v_mfma_f32_32x32x16_bf16 v[32:47], v[80:83], v[208:211], v[32:47]\n"
    "s_add_u32 m0, s65, 73728\n"
    "s_add_u32 s94, s49, 45056\n"
    "buffer_load_dwordx4 v0, s[16:19], s94 offen sc1 lds\n"
    "s_add_u32 m0, s65, 81920\n"
    "s_add_u32 s94, s49, 53248\n"
    "buffer_load_dwordx4 v0, s[16:19], s94 offen sc1 lds\n"
    "ds_read_b128 v[228:231], v9 offset:0\n"
    "ds_read_b64_tr_b16 v[100:101], v12 offset:18432\n"
    "ds_read_b64_tr_b16 v[102:103], v12 offset:18944\n"
    "s_waitcnt lgkmcnt(12)\n"
    "v_mfma_f32_32x32x16_bf16 v[32:47], v[84:87], v[212:215], v[32:47]\n"
    "ds_read_b128 v[232:235], v10 offset:0\n"
    "ds_read_b64_tr_b16 v[104:105], v12 offset:20480\n"
    "ds_read_b64_tr_b16 v[106:107], v12 offset:20992\n"
    "s_waitcnt lgkmcnt(12)\n"
    "v_mfma_f32_32x32x16_bf16 v[32:47], v[88:91], v[216:219], v[32:47]\n"
    "ds_read_b128 v[236:239], v11 offset:0\n"
    "ds_read_b64_tr_b16 v[108:109], v12 offset:22528\n"
    "ds_read_b64_tr_b16 v[110:111], v12 offset:23040\n"
    "s_waitcnt lgkmcnt(12)\n"
    "v_mfma_f32_32x32x16_bf16 v[32:47], v[92:95], v[220:223], v[32:47]\n"
    "v_add_u32 v8, 98304, v8\n"
    "v_add_u32 v9, 98304, v9\n"
    "v_add_u32 v10, 98304, v10\n"
    "v_add_u32 v11, 98304, v11\n"
    "v_add_u32 v12, 98304, v12\n"
    "s_waitcnt vmcnt(7) lgkmcnt(0)\n"
    "s_barrier\n"
    "s_sleep 2\n"
    "s_add_u32 m0, s65, 90112\n"
    "s_add_u32 s94, s49, 61440\n"
    "buffer_load_dwordx4 v0, s[16:19], s94 offen sc1 lds\n"
    "ds_read_b128 v[240:243], v8 offset:0\n"
    "ds_read_b64_tr_b16 v[80:81], v12 offset:16384\n"
    "ds_read_b64_tr_b16 v[82:83], v12 offset:16896\n"
    "s_waitcnt lgkmcnt(12)\n"
    "v_mfma_f32_32x32x16_bf16 v[32:47], v[96:99], v[224:227], v[32:47]\n"
    "s_add_u32 m0, s65, 98304\n"
    "s_add_u32 s94, s49, 69632\n"
    "buffer_load_dwordx4 v0, s[16:19], s94 offen sc1 lds\n"
    "ds_read_b128 v[244:247], v9 offset:0\n"
    "ds_read_b64_tr_b16 v[84:85], v12 offset:18432\n"
    "ds_read_b64_tr_b16 v[86:87], v12 offset:18944\n"
    "s_waitcnt lgkmcnt(12)\n"
    "v_mfma_f32_32x32x16_bf16 v[32:47], v[100:103], v[228:231], v[32:47]\n"
    "s_add_u32 m0, s65, 106496\n"
    "s_add_u32 s94, s49, 77824\n"
    "buffer_load_dwordx4 v0, s[16:19], s94 offen sc1 lds\n"
    "ds_read_b128 v[248:251], v10 offset:0\n"
    "ds_read_b64_tr_b16 v[88:89], v12 offset:20480\n"
    "ds_read_b64_tr_b16 v[90:91], v12 offset:20992\n"
    "s_waitcnt lgkmcnt(12)\n"
    "v_mfma_f32_32x32x16_bf16 v[32:47], v[104:107], v[232:235], v[32:47]\n"
    "ds_read_b128 v[252:255], v11 offset:0\n"
    "ds_read_b64_tr_b16 v[92:93], v12 offset:22528\n"
    "ds_read_b64_tr_b16 v[94:95], v12 offset:23040\n"
    "s_waitcnt lgkmcnt(12)\n"
    "v_mfma_f32_32x32x16_bf16 v[32:47], v[108:111], v[236:239], v[32:47]\n"
    "v_xor_b32 v9, 16, v14\n"
    "v_xor_b32 v10, 32, v14\n"
    "v_xor_b32 v11, 48, v14\n"
    "v_add_u32 v12, 4294836224, v12\n"
    "s_waitcnt lgkmcnt(9)\n"
    "v_mfma_f32_32x32x16_bf16 v[32:47], v[80:83], v[240:243], v[32:47]\n"
    "s_waitcnt lgkmcnt(6)\n"
    "v_mfma_f32_32x32x16_bf16 v[32:47], v[84:87], v[244:247], v[32:47]\n"
    "s_waitcnt lgkmcnt(3)\n"
    "v_mfma_f32_32x32x16_bf16 v[32:47], v[88:91], v[248:251], v[32:47]\n"
    "s_waitcnt lgkmcnt(0)\n"
    "v_mfma_f32_32x32x16_bf16 v[32:47], v[92:95], v[252:255], v[32:47]\n"
    "s_cmp_eq_u32 s57, 0\n"
    "s_waitcnt vmcnt(3)\n"
    "s_barrier\n"
    "s_cbranch_scc0 .Lch1_%=\n"
    "ds_read_b64_tr_b16 v[80:81], v13 offset:0\n"
    "ds_read_b64_tr_b16 v[82:83], v13 offset:256\n"
    "ds_read_b64_tr_b16 v[84:85], v13 offset:1024\n"
    "ds_read_b64_tr_b16 v[86:87], v13 offset:1280\n"
    "ds_read_b64_tr_b16 v[88:89], v13 offset:2048\n"
    "ds_read_b64_tr_b16 v[90:91], v13 offset:2304\n"
    "ds_read_b64_tr_b16 v[92:93], v13 offset:3072\n"
    "ds_read_b64_tr_b16 v[94:95], v13 offset:3328\n"
    "ds_read_b64_tr_b16 v[96:97], v13 offset:4096\n"
    "ds_read_b64_tr_b16 v[98:99], v13 offset:4352\n"
    "s_waitcnt lgkmcnt(8)\n"
    "v_mfma_f32_32x32x16_bf16 v[48:63], v[80:83], v[144:147], 0\n"
    "s_nop 4\n"
    "ds_read_b64_tr_b16 v[100:101], v13 offset:5120\n"
    "ds_read_b64_tr_b16 v[102:103], v13 offset:5376\n"
    "s_waitcnt lgkmcnt(8)\n"
    "v_mfma_f32_32x32x16_bf16 v[48:63], v[84:87], v[148:151], v[48:63]\n"
    "s_nop 4\n"
    "ds_read_b64_tr_b16 v[80:81], v13 offset:6144\n"
    "ds_read_b64_tr_b16 v[82:83], v13 offset:6400\n"
    "s_waitcnt lgkmcnt(8)\n"
    "v_mfma_f32_32x32x16_bf16 v[48:63], v[88:91], v[152:155], v[48:63]\n"
    "s_nop 4\n"
    "ds_read_b64_tr_b16 v[84:85], v13 offset:7168\n"
    "ds_read_b64_tr_b16 v[86:87], v13 offset:7424\n"
    "s_waitcnt lgkmcnt(8)\n"
    "v_mfma_f32_32x32x16_bf16 v[48:63], v[92:95], v[156:159], v[48:63]\n"
    "s_nop 4\n"
    "ds_read_b64_tr_b16 v[88:89], v13 offset:8192\n"
    "ds_read_b64_tr_b16 v[90:91], v13 offset:8448\n"
    "s_waitcnt lgkmcnt(8)\n"
    "v_mfma_f32_32x32x16_bf16 v[48:63], v[96:99], v[160:163], v[48:63]\n"
    "s_nop 4\n"
    "ds_read_b64_tr_b16 v[92:93], v13 offset:9216\n"
    "ds_read_b64_tr_b16 v[94:95], v13 offset:9472\n"
    "s_waitcnt lgkmcnt(8)\n"
    "v_mfma_f32_32x32x16_bf16 v[48:63], v[100:103], v[164:167], v[48:63]\n"
    "s_nop 4\n"
    "ds_read_b64_tr_b16 v[96:97], v13 offset:10240\n"
    "ds_read_b64_tr_b16 v[98:99], v13 offset:10496\n"
    "s_waitcnt lgkmcnt(8)\n"
    "v_mfma_f32_32x32x16_bf16 v[48:63], v[80:83], v[168:171], v[48:63]\n"
    "s_nop 4\n"
    "ds_read_b64_tr_b16 v[100:101], v13 offset:11264\n"
    "ds_read_b64_tr_b16 v[102:103], v13 offset:11520\n"
    "s_waitcnt lgkmcnt(8)\n"
    "v_mfma_f32_32x32x16_bf16 v[48:63], v[84:87], v[172:175], v[48:63]\n"
    "s_nop 4\n"
    "ds_read_b64_tr_b16 v[80:81], v13 offset:12288\n"
    "ds_read_b64_tr_b16 v[82:83], v13 offset:12544\n"
    "s_waitcnt lgkmcnt(8)\n"
    "v_mfma_f32_32x32x16_bf16 v[48:63], v[88:91], v[176:179], v[48:63]\n"
    "s_nop 4\n"
    "ds_read_b64_tr_b16 v[84:85], v13 offset:13312\n"
    "ds_read_b64_tr_b16 v[86:87], v13 offset:13568\n"
    "s_waitcnt lgkmcnt(8)\n"
    "v_mfma_f32_32x32x16_bf16 v[48:63], v[92:95], v[180:183], v[48:63]\n"
    "s_nop 4\n"
    "ds_read_b64_tr_b16 v[88:89], v13 offset:14336\n"
    "ds_read_b64_tr_b16 v[90:91], v13 offset:14592\n"
    "s_waitcnt lgkmcnt(8)\n"
    "v_mfma_f32_32x32x16_bf16 v[48:63], v[96:99], v[184:187], v[48:63]\n"
    "s_nop 4\n"
    "ds_read_b64_tr_b16 v[92:93], v13 offset:15360\n"
    "ds_read_b64_tr_b16 v[94:95], v13 offset:15616\n"
    "s_waitcnt lgkmcnt(8)\n"
    "v_mfma_f32_32x32x16_bf16 v[48:63], v[100:103], v[188:191], v[48:63]\n"
    "s_nop 4\n"
    "ds_read_b64_tr_b16 v[96:97], v13 offset:16384\n"
    "ds_read_b64_tr_b16 v[98:99], v13 offset:16640\n"
    "s_waitcnt lgkmcnt(8)\n"
    "v_mfma_f32_32x32x16_bf16 v[48:63], v[80:83], v[192:195], v[48:63]\n"
    "s_nop 4\n"
    "ds_read_b64_tr_b16 v[100:101], v13 offset:17408\n"
    "ds_read_b64_tr_b16 v[102:103], v13 offset:17664\n"
    "s_waitcnt lgkmcnt(8)\n"
    "v_mfma_f32_32x32x16_bf16 v[48:63], v[84:87], v[196:199], v[48:63]\n"
    "s_nop 4\n"
    "ds_read_b64_tr_b16 v[80:81], v13 offset:18432\n"
    "ds_read_b64_tr_b16 v[82:83], v13 offset:18688\n"
    "s_waitcnt lgkmcnt(8)\n"
    "v_mfma_f32_32x32x16_bf16 v[48:63], v[88:91], v[200:203], v[48:63]\n"
    "s_nop 4\n"
    "ds_read_b64_tr_b16 v[84:85], v13 offset:19456\n"
    "ds_read_b64_tr_b16 v[86:87], v13 offset:19712\n"
    "s_waitcnt lgkmcnt(8)\n"
    "v_mfma_f32_32x32x16_bf16 v[48:63], v[92:95], v[204:207], v[48:63]\n"
    "s_nop 4\n"
    "ds_read_b64_tr_b16 v[88:89], v13 offset:20480\n"
    "ds_read_b64_tr_b16 v[90:91], v13 offset:20736\n"
    "s_waitcnt lgkmcnt(8)\n"
    "v_mfma_f32_32x32x16_bf16 v[48:63], v[96:99], v[208:211], v[48:63]\n"
    "s_nop 4\n"
    "ds_read_b64_tr_b16 v[92:93], v13 offset:21504\n"
    "ds_read_b64_tr_b16 v[94:95], v13 offset:21760\n"
    "s_waitcnt lgkmcnt(8)\n"
    "v_mfma_f32_32x32x16_bf16 v[48:63], v[100:103], v[212:215], v[48:63]\n"
    "s_nop 4\n"
    "ds_read_b64_tr_b16 v[96:97], v13 offset:22528\n"
    "ds_read_b64_tr_b16 v[98:99], v13 offset:22784\n"
    "s_waitcnt lgkmcnt(8)\n"
    "v_mfma_f32_32x32x16_bf16 v[48:63], v[80:83], v[216:219], v[48:63]\n"
    "s_nop 4\n"
    "ds_read_b64_tr_b16 v[100:101], v13 offset:23552\n"
    "ds_read_b64_tr_b16 v[102:103], v13 offset:23808\n"
    "s_waitcnt lgkmcnt(8)\n"
    "v_mfma_f32_32x32x16_bf16 v[48:63], v[84:87], v[220:223], v[48:63]\n"
    "s_nop 4\n"
    "ds_read_b64_tr_b16 v[80:81], v13 offset:24576\n"
    "ds_read_b64_tr_b16 v[82:83], v13 offset:24832\n"
    "s_waitcnt lgkmcnt(8)\n"
    "v_mfma_f32_32x32x16_bf16 v[48:63], v[88:91], v[224:227], v[48:63]\n"
    "s_nop 4\n"
    "ds_read_b64_tr_b16 v[84:85], v13 offset:25600\n"
    "ds_read_b64_tr_b16 v[86:87], v13 offset:25856\n"
    "s_waitcnt lgkmcnt(8)\n"
    "v_mfma_f32_32x32x16_bf16 v[48:63], v[92:95], v[228:231], v[48:63]\n"
    "s_nop 4\n"
    "ds_read_b64_tr_b16 v[88:89], v13 offset:26624\n"
    "ds_read_b64_tr_b16 v[90:91], v13 offset:26880\n"
    "s_waitcnt lgkmcnt(8)\n"
    "v_mfma_f32_32x32x16_bf16 v[48:63], v[96:99], v[232:235], v[48:63]\n"
    "s_nop 4\n"
    "ds_read_b64_tr_b16 v[92:93], v13 offset:27648\n"
    "ds_read_b64_tr_b16 v[94:95], v13 offset:27904\n"
    "s_waitcnt lgkmcnt(8)\n"
    "v_mfma_f32_32x32x16_bf16 v[48:63], v[100:103], v[236:239], v[48:63]\n"
    "s_nop 4\n"
    "s_waitcnt lgkmcnt(6)\n"
    "v_mfma_f32_32x32x16_bf16 v[48:63], v[80:83], v[240:243], v[48:63]\n"
    "s_nop 6\n"
    "s_waitcnt lgkmcnt(4)\n"
    "v_mfma_f32_32x32x16_bf16 v[48:63], v[84:87], v[244:247], v[48:63]\n"
    "s_nop 6\n"
    "s_waitcnt lgkmcnt(2)\n"
    "v_mfma_f32_32x32x16_bf16 v[48:63], v[88:91], v[248:251], v[48:63]\n"
    "s_nop 6\n"
    "s_waitcnt lgkmcnt(0)\n"
    "v_mfma_f32_32x32x16_bf16 v[48:63], v[92:95], v[252:255], v[48:63]\n"
    "ds_read_b64_tr_b16 v[128:129], v15 offset:0\n"
    "ds_read_b64_tr_b16 v[130:131], v15 offset:448\n"
    "ds_read_b64_tr_b16 v[132:133], v15 offset:896\n"
    "ds_read_b64_tr_b16 v[134:135], v15 offset:1344\n"
    "s_nop 7\n"
    "v_cvt_pk_bf16_f32 v48, v48, v49\n"
    "v_cvt_pk_bf16_f32 v49, v50, v51\n"
    "v_cvt_pk_bf16_f32 v50, v52, v53\n"
    "v_cvt_pk_bf16_f32 v51, v54, v55\n"
    "v_cvt_pk_bf16_f32 v52, v56, v57\n"
    "v_cvt_pk_bf16_f32 v53, v58, v59\n"
    "v_cvt_pk_bf16_f32 v54, v60, v61\n"
    "v_cvt_pk_bf16_f32 v55, v62, v63\n"
    "s_waitcnt lgkmcnt(0)\n"
    "v_cndmask_b32 v134, v134, 0, s[54:55]\n"
    "v_cndmask_b32 v135, v135, 0, s[54:55]\n"
    "v_mfma_f32_32x32x16_bf16 v[112:127], v[128:131], v[48:51], 0\n"
    "s_nop 0\n"
    "v_mfma_f32_32x32x16_bf16 v[112:127], v[132:135], v[52:55], v[112:127]\n"
    "s_waitcnt vmcnt(0)\n"
    "s_barrier\n"
    "s_setprio 0\n"
    "s_nop 8\n"
    "v_max3_f32 v22, v112, v113, v114\n"
    "v_max3_f32 v23, -v112, -v113, -v114\n"
    "v_max3_f32 v22, v22, v115, v116\n"
    "v_max3_f32 v23, v23, -v115, -v116\n"
    "v_max3_f32 v22, v22, v117, v118\n"
    "v_max3_f32 v23, v23, -v117, -v118\n"
    "v_max3_f32 v22, v22, v119, v120\n"
    "v_max3_f32 v23, v23, -v119, -v120\n"
    "v_max3_f32 v22, v22, v121, v122\n"
    "v_max3_f32 v23, v23, -v121, -v122\n"
    "v_max3_f32 v22, v22, v123, v124\n"
    "v_max3_f32 v23, v23, -v123, -v124\n"
    "v_max3_f32 v22, v22, v125, v126\n"
    "v_max3_f32 v23, v23, -v125, -v126\n"
    "v_max_f32 v22, v22, v127\n"
    "v_max_f32_e64 v23, v23, -v127\n"
    "s_nop 1\n"
    "v_permlane32_swap_b32 v22, v23\n"
    "v_max_f32 v22, v22, v23\n"
    "v_mul_f32 v22, 0xbfb8aa3b, v22\n"
    "v_mov_b32 v23, v22\n"
    "s_nop 1\n"
    "v_permlane32_swap_b32 v22, v23\n"
    "v_fma_f32 v80, v112, s87, v22\n"
    "v_fma_f32 v96, -v112, s87, v23\n"
    "v_fma_f32 v81, v113, s87, v22\n"
    "v_fma_f32 v97, -v113, s87, v23\n"
    "v_exp_f32 v80, v80\n"
    "v_exp_f32 v96, v96\n"
    "v_exp_f32 v81, v81\n"
    "v_exp_f32 v97, v97\n"
    "v_fma_f32 v82, v114, s87, v22\n"
    "v_fma_f32 v98, -v114, s87, v23\n"
    "v_fma_f32 v83, v115, s87, v22\n"
    "v_fma_f32 v99, -v115, s87, v23\n"
    "v_exp_f32 v82, v82\n"
    "v_exp_f32 v98, v98\n"
    "v_exp_f32 v83, v83\n"
    "v_exp_f32 v99, v99\n"
    "v_fma_f32 v84, v116, s87, v22\n"
    "v_fma_f32 v100, -v116, s87, v23\n"
    "v_fma_f32 v85, v117, s87, v22\n"
    "v_fma_f32 v101, -v117, s87, v23\n"
    "v_exp_f32 v84, v84\n"
    "v_exp_f32 v100, v100\n"
    "v_exp_f32 v85, v85\n"
    "v_exp_f32 v101, v101\n"
    "v_fma_f32 v86, v118, s87, v22\n"
    "v_fma_f32 v102, -v118, s87, v23\n"
    "v_fma_f32 v87, v119, s87, v22\n"
    "v_fma_f32 v103, -v119, s87, v23\n"
    "v_exp_f32 v86, v86\n"
    "v_exp_f32 v102, v102\n"
    "v_exp_f32 v87, v87\n"
    "v_exp_f32 v103, v103\n"
    "v_fma_f32 v88, v120, s87, v22\n"
    "v_fma_f32 v104, -v120, s87, v23\n"
    "v_fma_f32 v89, v121, s87, v22\n"
    "v_fma_f32 v105, -v121, s87, v23\n"
    "v_exp_f32 v88, v88\n"
    "v_exp_f32 v104, v104\n"
    "v_exp_f32 v89, v89\n"
    "v_exp_f32 v105, v105\n"
    "v_fma_f32 v90, v122, s87, v22\n"
    "v_fma_f32 v106, -v122, s87, v23\n"
    "v_fma_f32 v91, v123, s87, v22\n"
    "v_fma_f32 v107, -v123, s87, v23\n"
    "v_exp_f32 v90, v90\n"
    "v_exp_f32 v106, v106\n"
    "v_exp_f32 v91, v91\n"
    "v_exp_f32 v107, v107\n"
    "v_fma_f32 v92, v124, s87, v22\n"
    "v_fma_f32 v108, -v124, s87, v23\n"
    "v_fma_f32 v93, v125, s87, v22\n"
    "v_fma_f32 v109, -v125, s87, v23\n"
    "v_exp_f32 v92, v92\n"
    "v_exp_f32 v108, v108\n"
    "v_exp_f32 v93, v93\n"
    "v_exp_f32 v109, v109\n"
    "v_fma_f32 v94, v126, s87, v22\n"
    "v_fma_f32 v110, -v126, s87, v23\n"
    "v_fma_f32 v95, v127, s87, v22\n"
    "v_fma_f32 v111, -v127, s87, v23\n"
    "v_exp_f32 v94, v94\n"
    "v_exp_f32 v110, v110\n"
    "v_exp_f32 v95, v95\n"
    "v_exp_f32 v111, v111\n"
    "v_add_f32 v24, v92, v93\n"
    "v_add_f32 v25, v108, v109\n"
    "v_add_f32 v22, v80, v81\n"
    "v_add_f32 v23, v96, v97\n"
    "v_add_f32 v24, v24, v94\n"
    "v_add_f32 v25, v25, v110\n"
    "v_add_f32 v24, v24, v95\n"
    "v_add_f32 v25, v25, v111\n"
    "v_cndmask_b32 v24, v24, 0, s[54:55]\n"
    "v_cndmask_b32 v25, v25, 0, s[54:55]\n"
    "v_add_f32 v22, v22, v82\n"
    "v_add_f32 v23, v23, v98\n"
    "v_add_f32 v24, v24, v83\n"
    "v_add_f32 v25, v25, v99\n"
    "v_add_f32 v22, v22, v84\n"
    "v_add_f32 v23, v23, v100\n"
    "v_add_f32 v24, v24, v85\n"
    "v_add_f32 v25, v25, v101\n"
    "v_add_f32 v22, v22, v86\n"
    "v_add_f32 v23, v23, v102\n"
    "v_add_f32 v24, v24, v87\n"
    "v_add_f32 v25, v25, v103\n"
    "v_add_f32 v22, v22, v88\n"
    "v_add_f32 v23, v23, v104\n"
    "v_add_f32 v24, v24, v89\n"
    "v_add_f32 v25, v25, v105\n"
    "v_add_f32 v22, v22, v90\n"
    "v_add_f32 v23, v23, v106\n"
    "v_add_f32 v24, v24, v91\n"
    "v_add_f32 v25, v25, v107\n"
    "v_add_f32 v22, v22, v24\n"
    "v_add_f32 v23, v23, v25\n"
    "v_xor_b32 v1, 16, v140\n"
    "s_lshl_b32 s70, s58, 13\n"
    "v_permlane32_swap_b32 v22, v23\n"
    "v_add_f32 v22, v22, v23\n"
    "v_rcp_f32 v22, v22\n"
    "v_add_u32 v8, s70, v140\n"
    "v_mov_b32 v23, v22\n"
    "v_add_u32 v2, s70, v1\n"
    "s_nop 0\n"
    "v_permlane32_swap_b32 v22, v23\n"
    "v_mul_f32 v80, v22, v80\n"
    "v_mul_f32 v81, v22, v81\n"
    "v_mul_f32 v82, v22, v82\n"
    "v_mul_f32 v83, v22, v83\n"
    "v_cvt_pk_bf16_f32 v80, v80, v81\n"
    "v_cvt_pk_bf16_f32 v81, v82, v83\n"
    "ds_write_b64 v14, v[80:81] offset:0\n"
    "v_mul_f32 v96, v23, v96\n"
    "v_mul_f32 v97, v23, v97\n"
    "v_mul_f32 v98, v23, v98\n"
    "v_mul_f32 v99, v23, v99\n"
    "v_cvt_pk_bf16_f32 v96, v96, v97\n"
    "v_cvt_pk_bf16_f32 v97, v98, v99\n"
    "ds_write_b64 v14, v[96:97] offset:8192\n"
    "v_mul_f32 v84, v22, v84\n"
    "v_mul_f32 v85, v22, v85\n"
    "v_mul_f32 v86, v22, v86\n"
    "v_mul_f32 v87, v22, v87\n"
    "v_cvt_pk_bf16_f32 v84, v84, v85\n"
    "v_cvt_pk_bf16_f32 v85, v86, v87\n"
    "ds_write_b64 v9, v[84:85] offset:0\n"
    "v_mul_f32 v100, v23, v100\n"
    "v_mul_f32 v101, v23, v101\n"
    "v_mul_f32 v102, v23, v102\n"
    "v_mul_f32 v103, v23, v103\n"
    "v_cvt_pk_bf16_f32 v100, v100, v101\n"
    "v_cvt_pk_bf16_f32 v101, v102, v103\n"
    "ds_write_b64 v9, v[100:101] offset:8192\n"
    "v_mul_f32 v88, v22, v88\n"
    "v_mul_f32 v89, v22, v89\n"
    "v_mul_f32 v90, v22, v90\n"
    "v_mul_f32 v91, v22, v91\n"
    "v_cvt_pk_bf16_f32 v88, v88, v89\n"
    "v_cvt_pk_bf16_f32 v89, v90, v91\n"
    "ds_write_b64 v10, v[88:89] offset:0\n"
    "v_mul_f32 v104, v23, v104\n"
    "v_mul_f32 v105, v23, v105\n"
    "v_mul_f32 v106, v23, v106\n"
    "v_mul_f32 v107, v23, v107\n"
    "v_cvt_pk_bf16_f32 v104, v104, v105\n"
    "v_cvt_pk_bf16_f32 v105, v106, v107\n"
    "ds_write_b64 v10, v[104:105] offset:8192\n"
    "v_mul_f32 v92, v22, v92\n"
    "v_mul_f32 v93, v22, v93\n"
    "v_mul_f32 v94, v22, v94\n"
    "v_mul_f32 v95, v22, v95\n"
    "v_cvt_pk_bf16_f32 v92, v92, v93\n"
    "v_cvt_pk_bf16_f32 v93, v94, v95\n"
    "ds_write_b64 v11, v[92:93] offset:0\n"
    "v_mul_f32 v108, v23, v108\n"
    "v_mul_f32 v109, v23, v109\n"
    "v_mul_f32 v110, v23, v110\n"
    "v_mul_f32 v111, v23, v111\n"
    "v_cvt_pk_bf16_f32 v108, v108, v109\n"
    "v_cvt_pk_bf16_f32 v109, v110, v111\n"
    "ds_write_b64 v11, v[108:109] offset:8192\n"
    "s_cmp_lt_u32 s58, 2\n"
    "s_waitcnt vmcnt(7) lgkmcnt(0)\n"
    "s_barrier\n"
    "s_cbranch_scc0 .Lnokv_%=\n"
    "s_setprio 2\n"
    "ds_read_b64_tr_b16 v[80:81], v8 offset:0\n"
    "ds_read_b64_tr_b16 v[82:83], v2 offset:256\n"
    "ds_read_b64_tr_b16 v[84:85], v140 offset:16384\n"
    "ds_read_b64_tr_b16 v[86:87], v1 offset:16640\n"
    "ds_read_b64_tr_b16 v[88:89], v8 offset:1024\n"
    "ds_read_b64_tr_b16 v[90:91], v2 offset:1280\n"
    "ds_read_b64_tr_b16 v[92:93], v140 offset:17408\n"
    "ds_read_b64_tr_b16 v[94:95], v1 offset:17664\n"
    "ds_read_b64_tr_b16 v[96:97], v8 offset:2048\n"
    "ds_read_b64_tr_b16 v[98:99], v2 offset:2304\n"
    "ds_read_b64_tr_b16 v[100:101], v140 offset:18432\n"
    "ds_read_b64_tr_b16 v[102:103], v1 offset:18688\n"
    "ds_read_b64_tr_b16 v[104:105], v8 offset:3072\n"
    "ds_read_b64_tr_b16 v[106:107], v2 offset:3328\n"
    "ds_read_b64_tr_b16 v[108:109], v140 offset:19456\n"
    "ds_read_b64_tr_b16 v[110:111], v1 offset:19712\n"
    "s_waitcnt lgkmcnt(12)\n"
    "v_mfma_f32_32x32x16_bf16 v[48:63], v[80:83], v[84:87], 0\n"
    "ds_read_b64_tr_b16 v[80:81], v8 offset:4096\n"
    "ds_read_b64_tr_b16 v[82:83], v2 offset:4352\n"
    "ds_read_b64_tr_b16 v[84:85], v140 offset:20480\n"
    "ds_read_b64_tr_b16 v[86:87], v1 offset:20736\n"
    "s_waitcnt lgkmcnt(12)\n"
    "v_mfma_f32_32x32x16_bf16 v[48:63], v[88:91], v[92:95], v[48:63]\n"
    "ds_read_b64_tr_b16 v[88:89], v8 offset:5120\n"
    "ds_read_b64_tr_b16 v[90:91], v2 offset:5376\n"
    "ds_read_b64_tr_b16 v[92:93], v140 offset:21504\n"
    "ds_read_b64_tr_b16 v[94:95], v1 offset:21760\n"
    "s_waitcnt lgkmcnt(12)\n"
    "v_mfma_f32_32x32x16_bf16 v[48:63], v[96:99], v[100:103], v[48:63]\n"
    "ds_read_b64_tr_b16 v[96:97], v8 offset:6144\n"
    "ds_read_b64_tr_b16 v[98:99], v2 offset:6400\n"
    "ds_read_b64_tr_b16 v[100:101], v140 offset:22528\n"
    "ds_read_b64_tr_b16 v[102:103], v1 offset:22784\n"
    "s_waitcnt lgkmcnt(12)\n"
    "v_mfma_f32_32x32x16_bf16 v[48:63], v[104:107], v[108:111], v[48:63]\n"
    "ds_read_b64_tr_b16 v[104:105], v8 offset:7168\n"
    "ds_read_b64_tr_b16 v[106:107], v2 offset:7424\n"
    "ds_read_b64_tr_b16 v[108:109], v140 offset:23552\n"
    "ds_read_b64_tr_b16 v[110:111], v1 offset:23808\n"
    "s_waitcnt lgkmcnt(12)\n"
    "v_mfma_f32_32x32x16_bf16 v[48:63], v[80:83], v[84:87], v[48:63]\n"
    "s_waitcnt lgkmcnt(8)\n"
    "v_mfma_f32_32x32x16_bf16 v[48:63], v[88:91], v[92:95], v[48:63]\n"
    "s_waitcnt lgkmcnt(4)\n"
    "v_mfma_f32_32x32x16_bf16 v[48:63], v[96:99], v[100:103], v[48:63]\n"
    "s_waitcnt lgkmcnt(0)\n"
    "v_mfma_f32_32x32x16_bf16 v[48:63], v[104:107], v[108:111], v[48:63]\n"
    "v_mov_b32 v22, 0xff\n"
    "s_nop 10\n"
    "v_bfi_b32 v48, v22, s12, v48\n"
    "v_bfi_b32 v49, v22, s13, v49\n"
    "v_bfi_b32 v50, v22, s14, v50\n"
    "v_bfi_b32 v51, v22, s15, v51\n"
    "buffer_store_dwordx4 v[48:51], v0, s[16:19], s83 offen offset:0 sc1\n"
    "v_bfi_b32 v52, v22, s12, v52\n"
    "v_bfi_b32 v53, v22, s13, v53\n"
    "v_bfi_b32 v54, v22, s14, v54\n"
    "v_bfi_b32 v55, v22, s15, v55\n"
    "buffer_store_dwordx4 v[52:55], v0, s[16:19], s83 offen offset:1024 sc1\n"
    "v_bfi_b32 v56, v22, s12, v56\n"
    "v_bfi_b32 v57, v22, s13, v57\n"
    "v_bfi_b32 v58, v22, s14, v58\n"
    "v_bfi_b32 v59, v22, s15, v59\n"
    "buffer_store_dwordx4 v[56:59], v0, s[16:19], s83 offen offset:2048 sc1\n"
    "v_bfi_b32 v60, v22, s12, v60\n"
    "v_bfi_b32 v61, v22, s13, v61\n"
    "v_bfi_b32 v62, v22, s14, v62\n"
    "v_bfi_b32 v63, v22, s15, v63\n"
    "buffer_store_dwordx4 v[60:63], v0, s[16:19], s83 offen offset:3072 sc1\n"
    "buffer_store_dwordx4 v[16:19], v7, s[16:19], s84 offen offset:0 sc1\n"
    "s_setprio 0\n"
    ".Lnokv_%=:\n"
    "s_cmp_eq_u32 s64, 1\n"
    "s_cbranch_scc1 .Lsp0_%=\n"
    "s_barrier\n"
    "v_mul_f32 v22, v32, v32\n"
    "v_mul_f32 v23, v33, v33\n"
    "v_fma_f32 v22, v22, s88, v21\n"
    "v_fma_f32 v23, v23, s88, v21\n"
    "v_mul_f32 v22, v32, v22\n"
    "v_mul_f32 v23, v33, v23\n"
    "v_exp_f32 v22, v22\n"
    "v_exp_f32 v23, v23\n"
    "v_mul_f32 v24, v34, v34\n"
    "v_mul_f32 v25, v35, v35\n"
    "v_add_f32 v22, 1.0, v22\n"
    "v_add_f32 v23, 1.0, v23\n"
    "v_rcp_f32 v22, v22\n"
    "v_rcp_f32 v23, v23\n"
    "v_fma_f32 v24, v24, s88, v21\n"
    "v_fma_f32 v25, v25, s88, v21\n"
    "v_mul_f32 v22, v32, v22\n"
    "v_mul_f32 v23, v33, v23\n"
    "v_cvt_pk_bf16_f32 v48, v22, v23\n"
    "v_mul_f32 v24, v34, v24\n"
    "v_mul_f32 v25, v35, v25\n"
    "v_exp_f32 v24, v24\n"
    "v_exp_f32 v25, v25\n"
    "v_mul_f32 v22, v36, v36\n"
    "v_mul_f32 v23, v37, v37\n"
    "v_add_f32 v24, 1.0, v24\n"
    "v_add_f32 v25, 1.0, v25\n"
    "v_rcp_f32 v24, v24\n"
    "v_rcp_f32 v25, v25\n"
    "v_fma_f32 v22, v22, s88, v21\n"
    "v_fma_f32 v23, v23, s88, v21\n"
    "v_mul_f32 v24, v34, v24\n"
    "v_mul_f32 v25, v35, v25\n"
    "v_cvt_pk_bf16_f32 v49, v24, v25\n"
    "v_mul_f32 v22, v36, v22\n"
    "v_mul_f32 v23, v37, v23\n"
    "v_exp_f32 v22, v22\n"
    "v_exp_f32 v23, v23\n"
    "v_mul_f32 v24, v38, v38\n"
    "v_mul_f32 v25, v39, v39\n"
    "v_add_f32 v22, 1.0, v22\n"
    "v_add_f32 v23, 1.0, v23\n"
    "v_rcp_f32 v22, v22\n"
    "v_rcp_f32 v23, v23\n"
    "v_fma_f32 v24, v24, s88, v21\n"
    "v_fma_f32 v25, v25, s88, v21\n"
    "v_mul_f32 v22, v36, v22\n"
    "v_mul_f32 v23, v37, v23\n"
    "v_cvt_pk_bf16_f32 v50, v22, v23\n"
    "v_mul_f32 v24, v38, v24\n"
    "v_mul_f32 v25, v39, v25\n"
    "v_exp_f32 v24, v24\n"
    "v_exp_f32 v25, v25\n"
    "v_mul_f32 v22, v40, v40\n"
    "v_mul_f32 v23, v41, v41\n"
    "v_add_f32 v24, 1.0, v24\n"
    "v_add_f32 v25, 1.0, v25\n"
    "v_rcp_f32 v24, v24\n"
    "v_rcp_f32 v25, v25\n"
    "v_fma_f32 v22, v22, s88, v21\n"
    "v_fma_f32 v23, v23, s88, v21\n"
    "v_mul_f32 v24, v38, v24\n"
    "v_mul_f32 v25, v39, v25\n"
    "v_cvt_pk_bf16_f32 v51, v24, v25\n"
    "v_mul_f32 v22, v40, v22\n"
    "v_mul_f32 v23, v41, v23\n"
    "v_exp_f32 v22, v22\n"
    "v_exp_f32 v23, v23\n"
    "v_mul_f32 v24, v42, v42\n"
    "v_mul_f32 v25, v43, v43\n"
    "v_add_f32 v22, 1.0, v22\n"
    "v_add_f32 v23, 1.0, v23\n"
    "s_add_u32 m0, s85, 99328\n"
    "s_add_u32 s70, s74, 7168\n"
    "buffer_load_dwordx4 v5, s[20:23], s70 offen lds\n"
    "v_rcp_f32 v22, v22\n"
    "v_rcp_f32 v23, v23\n"
    "v_fma_f32 v24, v24, s88, v21\n"
    "v_fma_f32 v25, v25, s88, v21\n"
    "v_mul_f32 v22, v40, v22\n"
    "v_mul_f32 v23, v41, v23\n"
    "v_cvt_pk_bf16_f32 v52, v22, v23\n"
    "v_mul_f32 v24, v42, v24\n"
    "v_mul_f32 v25, v43, v25\n"
    "v_exp_f32 v24, v24\n"
    "v_exp_f32 v25, v25\n"
    "v_mul_f32 v22, v44, v44\n"
    "s_add_u32 m0, s85, 106496\n"
    "s_add_u32 s70, s75, 0\n"
    "buffer_load_dwordx4 v5, s[20:23], s70 offen lds\n"
    "v_mul_f32 v23, v45, v45\n"
    "v_add_f32 v24, 1.0, v24\n"
    "v_add_f32 v25, 1.0, v25\n"
    "v_rcp_f32 v24, v24\n"
    "v_rcp_f32 v25, v25\n"
    "v_fma_f32 v22, v22, s88, v21\n"
    "v_fma_f32 v23, v23, s88, v21\n"
    "v_mul_f32 v24, v42, v24\n"
    "v_mul_f32 v25, v43, v25\n"
    "v_cvt_pk_bf16_f32 v53, v24, v25\n"
    "v_mul_f32 v22, v44, v22\n"
    "v_mul_f32 v23, v45, v23\n"
    "s_add_u32 m0, s85, 107520\n"
    "s_add_u32 s70, s75, 7168\n"
    "buffer_load_dwordx4 v5, s[20:23], s70 offen lds\n"
    "v_exp_f32 v22, v22\n"
    "v_exp_f32 v23, v23\n"
    "v_mul_f32 v24, v46, v46\n"
    "v_mul_f32 v25, v47, v47\n"
    "v_add_f32 v22, 1.0, v22\n"
    "v_add_f32 v23, 1.0, v23\n"
    "v_rcp_f32 v22, v22\n"
    "v_rcp_f32 v23, v23\n"
    "v_fma_f32 v24, v24, s88, v21\n"
    "v_fma_f32 v25, v25, s88, v21\n"
    "v_mul_f32 v22, v44, v22\n"
    "v_mul_f32 v23, v45, v23\n"
    "v_cvt_pk_bf16_f32 v54, v22, v23\n"
    "v_mul_f32 v24, v46, v24\n"
    "v_mul_f32 v25, v47, v25\n"
    "v_exp_f32 v24, v24\n"
    "v_exp_f32 v25, v25\n"
    "v_add_f32 v24, 1.0, v24\n"
    "v_add_f32 v25, 1.0, v25\n"
    "v_rcp_f32 v24, v24\n"
    "v_rcp_f32 v25, v25\n"
    "v_mul_f32 v24, v46, v24\n"
    "v_mul_f32 v25, v47, v25\n"
    "v_cvt_pk_bf16_f32 v55, v24, v25\n"
    "s_add_u32 s71, s79, 14336\n"
    "buffer_store_dwordx4 v[48:51], v0, s[16:19], s71 offen offset:0 sc1\n"
    "buffer_store_dwordx4 v[52:55], v0, s[16:19], s71 offen offset:1024 sc1\n"
    "ds_read_b64_tr_b16 v[80:81], v12 offset:0\n"
    "ds_read_b64_tr_b16 v[82:83], v12 offset:512\n"
    "ds_read_b64_tr_b16 v[84:85], v12 offset:2048\n"
    "ds_read_b64_tr_b16 v[86:87], v12 offset:2560\n"
    "ds_read_b64_tr_b16 v[88:89], v12 offset:4096\n"
    "ds_read_b64_tr_b16 v[90:91], v12 offset:4608\n"
    "ds_read_b64_tr_b16 v[92:93], v12 offset:6144\n"
    "ds_read_b64_tr_b16 v[94:95], v12 offset:6656\n"
    "ds_read_b64_tr_b16 v[96:97], v12 offset:8192\n"
    "ds_read_b64_tr_b16 v[98:99], v12 offset:8704\n"
    "s_waitcnt lgkmcnt(8)\n"
    "v_mfma_f32_32x32x16_bf16 v[64:79], v[80:83], v[144:147], v[64:79]\n"
    "s_nop 3\n"
    "ds_read_b64_tr_b16 v[100:101], v12 offset:10240\n"
    "ds_read_b64_tr_b16 v[102:103], v12 offset:10752\n"
    "s_waitcnt lgkmcnt(8)\n"
    "v_mfma_f32_32x32x16_bf16 v[64:79], v[84:87], v[148:151], v[64:79]\n"
    "s_nop 3\n"
    "ds_read_b64_tr_b16 v[80:81], v12 offset:12288\n"
    "ds_read_b64_tr_b16 v[82:83], v12 offset:12800\n"
    "s_waitcnt lgkmcnt(8)\n"
    "v_mfma_f32_32x32x16_bf16 v[64:79], v[88:91], v[152:155], v[64:79]\n"
    "s_nop 3\n"
    "ds_read_b64_tr_b16 v[84:85], v12 offset:14336\n"
    "ds_read_b64_tr_b16 v[86:87], v12 offset:14848\n"
    "s_waitcnt lgkmcnt(8)\n"
    "v_mfma_f32_32x32x16_bf16 v[64:79], v[92:95], v[156:159], v[64:79]\n"
    "s_nop 3\n"
    "ds_read_b64_tr_b16 v[88:89], v12 offset:16384\n"
    "ds_read_b64_tr_b16 v[90:91], v12 offset:16896\n"
    "s_waitcnt lgkmcnt(8)\n"
    "v_mfma_f32_32x32x16_bf16 v[64:79], v[96:99], v[160:163], v[64:79]\n"
    "s_nop 3\n"
    "ds_read_b64_tr_b16 v[92:93], v12 offset:18432\n"
    "ds_read_b64_tr_b16 v[94:95], v12 offset:18944\n"
    "s_waitcnt lgkmcnt(8)\n"
    "v_mfma_f32_32x32x16_bf16 v[64:79], v[100:103], v[164:167], v[64:79]\n"
    "s_nop 3\n"
    "ds_read_b64_tr_b16 v[96:97], v12 offset:20480\n"
    "ds_read_b64_tr_b16 v[98:99], v12 offset:20992\n"
    "s_waitcnt lgkmcnt(8)\n"
    "v_mfma_f32_32x32x16_bf16 v[64:79], v[80:83], v[168:171], v[64:79]\n"
    "s_nop 3\n"
    "ds_read_b64_tr_b16 v[100:101], v12 offset:22528\n"
    "ds_read_b64_tr_b16 v[102:103], v12 offset:23040\n"
    "s_waitcnt lgkmcnt(8)\n"
    "v_mfma_f32_32x32x16_bf16 v[64:79], v[84:87], v[172:175], v[64:79]\n"
    "s_nop 3\n"
    "ds_read_b64_tr_b16 v[80:81], v12 offset:24576\n"
    "ds_read_b64_tr_b16 v[82:83], v12 offset:25088\n"
    "s_waitcnt lgkmcnt(8)\n"
    "v_mfma_f32_32x32x16_bf16 v[64:79], v[88:91], v[176:179], v[64:79]\n"
    "s_add_u32 m0, s85, 73728\n"
    "s_add_u32 s70, s78, 0\n"
    "buffer_load_dwordx4 v5, s[20:23], s70 offen lds\n"
    "ds_read_b64_tr_b16 v[84:85], v12 offset:26624\n"
    "ds_read_b64_tr_b16 v[86:87], v12 offset:27136\n"
    "s_waitcnt lgkmcnt(8)\n"
    "v_mfma_f32_32x32x16_bf16 v[64:79], v[92:95], v[180:183], v[64:79]\n"
    "s_nop 3\n"
    "ds_read_b64_tr_b16 v[88:89], v12 offset:28672\n"
    "ds_read_b64_tr_b16 v[90:91], v12 offset:29184\n"
    "s_waitcnt lgkmcnt(8)\n"
    "v_mfma_f32_32x32x16_bf16 v[64:79], v[96:99], v[184:187], v[64:79]\n"
    "s_nop 3\n"
    "ds_read_b64_tr_b16 v[92:93], v12 offset:30720\n"
    "ds_read_b64_tr_b16 v[94:95], v12 offset:31232\n"
    "s_waitcnt lgkmcnt(8)\n"
    "v_mfma_f32_32x32x16_bf16 v[64:79], v[100:103], v[188:191], v[64:79]\n"
    "s_nop 3\n"
    "ds_read_b64_tr_b16 v[96:97], v12 offset:32768\n"
    "ds_read_b64_tr_b16 v[98:99], v12 offset:33280\n"
    "s_waitcnt lgkmcnt(8)\n"
    "v_mfma_f32_32x32x16_bf16 v[64:79], v[80:83], v[192:195], v[64:79]\n"
    "s_nop 3\n"
    "ds_read_b64_tr_b16 v[100:101], v12 offset:34816\n"
    "ds_read_b64_tr_b16 v[102:103], v12 offset:35328\n"
    "s_waitcnt lgkmcnt(8)\n"
    "v_mfma_f32_32x32x16_bf16 v[64:79], v[84:87], v[196:199], v[64:79]\n"
    "s_nop 3\n"
    "ds_read_b64_tr_b16 v[80:81], v12 offset:36864\n"
    "ds_read_b64_tr_b16 v[82:83], v12 offset:37376\n"
    "s_waitcnt lgkmcnt(8)\n"
    "v_mfma_f32_32x32x16_bf16 v[64:79], v[88:91], v[200:203], v[64:79]\n"
    "s_setprio 1\n"
    "s_add_u32 m0, s85, 58368\n"
    "s_add_u32 s70, s76, 7168\n"
    "buffer_load_dwordx4 v5, s[20:23], s70 offen lds\n"
    "ds_read_b64_tr_b16 v[84:85], v12 offset:38912\n"
    "ds_read_b64_tr_b16 v[86:87], v12 offset:39424\n"
    "s_waitcnt lgkmcnt(8)\n"
    "v_mfma_f32_32x32x16_bf16 v[64:79], v[92:95], v[204:207], v[64:79]\n"
    "s_nop 3\n"
    "ds_read_b64_tr_b16 v[88:89], v12 offset:40960\n"
    "ds_read_b64_tr_b16 v[90:91], v12 offset:41472\n"
    "s_waitcnt lgkmcnt(8)\n"
    "v_mfma_f32_32x32x16_bf16 v[64:79], v[96:99], v[208:211], v[64:79]\n"
    "s_nop 3\n"
    "ds_read_b64_tr_b16 v[92:93], v12 offset:43008\n"
    "ds_read_b64_tr_b16 v[94:95], v12 offset:43520\n"
    "s_waitcnt lgkmcnt(8)\n"
    "v_mfma_f32_32x32x16_bf16 v[64:79], v[100:103], v[212:215], v[64:79]\n"
    "s_nop 3\n"
    "ds_read_b64_tr_b16 v[96:97], v12 offset:45056\n"
    "ds_read_b64_tr_b16 v[98:99], v12 offset:45568\n"
    "s_waitcnt lgkmcnt(8)\n"
    "v_mfma_f32_32x32x16_bf16 v[64:79], v[80:83], v[216:219], v[64:79]\n"
    "s_nop 3\n"
    "ds_read_b64_tr_b16 v[100:101], v12 offset:47104\n"
    "ds_read_b64_tr_b16 v[102:103], v12 offset:47616\n"
    "s_waitcnt lgkmcnt(8)\n"
    "v_mfma_f32_32x32x16_bf16 v[64:79], v[84:87], v[220:223], v[64:79]\n"
    "s_add_u32 m0, s85, 81920\n"
    "s_add_u32 s70, s72, 0\n"
    "buffer_load_dwordx4 v5, s[20:23], s70 offen lds\n"
    "ds_read_b64_tr_b16 v[80:81], v12 offset:49152\n"
    "ds_read_b64_tr_b16 v[82:83], v12 offset:49664\n"
    "s_waitcnt lgkmcnt(8)\n"
    "v_mfma_f32_32x32x16_bf16 v[64:79], v[88:91], v[224:227], v[64:79]\n"
    "s_add_u32 m0, s85, 82944\n"
    "s_add_u32 s70, s72, 7168\n"
    "buffer_load_dwordx4 v5, s[20:23], s70 offen lds\n"
    "ds_read_b64_tr_b16 v[84:85], v12 offset:51200\n"
    "ds_read_b64_tr_b16 v[86:87], v12 offset:51712\n"
    "s_waitcnt lgkmcnt(8)\n"
    "v_mfma_f32_32x32x16_bf16 v[64:79], v[92:95], v[228:231], v[64:79]\n"
    "s_add_u32 m0, s85, 90112\n"
    "s_add_u32 s70, s73, 0\n"
    "buffer_load_dwordx4 v5, s[20:23], s70 offen lds\n"
    "ds_read_b64_tr_b16 v[88:89], v12 offset:53248\n"
    "ds_read_b64_tr_b16 v[90:91], v12 offset:53760\n"
    "s_waitcnt lgkmcnt(8)\n"
    "v_mfma_f32_32x32x16_bf16 v[64:79], v[96:99], v[232:235], v[64:79]\n"
    "s_add_u32 m0, s85, 91136\n"
    "s_add_u32 s70, s73, 7168\n"
    "buffer_load_dwordx4 v5, s[20:23], s70 offen lds\n"
    "ds_read_b64_tr_b16 v[92:93], v12 offset:55296\n"
    "ds_read_b64_tr_b16 v[94:95], v12 offset:55808\n"
    "s_waitcnt lgkmcnt(8)\n"
    "v_mfma_f32_32x32x16_bf16 v[64:79], v[100:103], v[236:239], v[64:79]\n"
    "s_nop 3\n"
    "s_waitcnt lgkmcnt(6)\n"
    "v_mfma_f32_32x32x16_bf16 v[64:79], v[80:83], v[240:243], v[64:79]\n"
    "s_add_u32 m0, s85, 98304\n"
    "s_add_u32 s70, s74, 0\n"
    "buffer_load_dwordx4 v5, s[20:23], s70 offen lds\n"
    "s_waitcnt lgkmcnt(4)\n"
    "v_mfma_f32_32x32x16_bf16 v[64:79], v[84:87], v[244:247], v[64:79]\n"
    "s_nop 5\n"
    "s_waitcnt lgkmcnt(2)\n"
    "v_mfma_f32_32x32x16_bf16 v[64:79], v[88:91], v[248:251], v[64:79]\n"
    "s_nop 5\n"
    "s_waitcnt lgkmcnt(0)\n"
    "v_mfma_f32_32x32x16_bf16 v[64:79], v[92:95], v[252:255], v[64:79]\n"
    "s_setprio 0\n"
    "s_waitcnt vmcnt(7) lgkmcnt(0)\n"
    "s_barrier\n"
    "buffer_store_dwordx4 v[16:19], v142, s[16:19], s80 offen offset:2048 sc1\n"
    "buffer_load_dwordx4 v[112:115], v0, s[16:19], s37 offen offset:0 sc1\n"
    "s_nop 6\n"
    "v_mul_f32 v22, v64, v64\n"
    "v_mul_f32 v23, v65, v65\n"
    "v_fma_f32 v22, v22, s88, v21\n"
    "v_fma_f32 v23, v23, s88, v21\n"
    "v_mul_f32 v22, v64, v22\n"
    "v_mul_f32 v23, v65, v23\n"
    "v_exp_f32 v22, v22\n"
    "v_exp_f32 v23, v23\n"
    "v_mul_f32 v24, v66, v66\n"
    "v_mul_f32 v25, v67, v67\n"
    "v_add_f32 v22, 1.0, v22\n"
    "v_add_f32 v23, 1.0, v23\n"
    "buffer_load_dwordx4 v[116:119], v0, s[16:19], s36 offen offset:0 sc1\n"
    "v_rcp_f32 v22, v22\n"
    "v_rcp_f32 v23, v23\n"
    "v_fma_f32 v24, v24, s88, v21\n"
    "v_fma_f32 v25, v25, s88, v21\n"
    "v_mul_f32 v22, v64, v22\n"
    "v_mul_f32 v23, v65, v23\n"
    "v_cvt_pk_bf16_f32 v32, v22, v23\n"
    "v_mul_f32 v24, v66, v24\n"
    "v_mul_f32 v25, v67, v25\n"
    "v_exp_f32 v24, v24\n"
    "v_exp_f32 v25, v25\n"
    "v_mul_f32 v22, v68, v68\n"
    "s_add_u32 m0, s85, 41984\n"
    "s_add_u32 s70, s77, 64512\n"
    "buffer_load_dwordx4 v5, s[20:23], s70 offen lds\n"
    "v_mul_f32 v23, v69, v69\n"
    "v_add_f32 v24, 1.0, v24\n"
    "v_add_f32 v25, 1.0, v25\n"
    "v_rcp_f32 v24, v24\n"
    "v_rcp_f32 v25, v25\n"
    "v_fma_f32 v22, v22, s88, v21\n"
    "v_fma_f32 v23, v23, s88, v21\n"
    "v_mul_f32 v24, v66, v24\n"
    "v_mul_f32 v25, v67, v25\n"
    "v_cvt_pk_bf16_f32 v33, v24, v25\n"
    "v_mul_f32 v22, v68, v22\n"
    "v_mul_f32 v23, v69, v23\n"
    "v_exp_f32 v22, v22\n"
    "v_exp_f32 v23, v23\n"
    "v_mul_f32 v24, v70, v70\n"
    "v_mul_f32 v25, v71, v71\n"
    "v_add_f32 v22, 1.0, v22\n"
    "v_add_f32 v23, 1.0, v23\n"
    "v_rcp_f32 v22, v22\n"
    "v_rcp_f32 v23, v23\n"
    "v_fma_f32 v24, v24, s88, v21\n"
    "v_fma_f32 v25, v25, s88, v21\n"
    "v_mul_f32 v22, v68, v22\n"
    "v_mul_f32 v23, v69, v23\n"
    "v_cvt_pk_bf16_f32 v34, v22, v23\n"
    "v_mul_f32 v24, v70, v24\n"
    "v_mul_f32 v25, v71, v25\n"
    "v_exp_f32 v24, v24\n"
    "v_exp_f32 v25, v25\n"
    "v_mul_f32 v22, v72, v72\n"
    "v_mul_f32 v23, v73, v73\n"
    "v_add_f32 v24, 1.0, v24\n"
    "v_add_f32 v25, 1.0, v25\n"
    "v_rcp_f32 v24, v24\n"
    "v_rcp_f32 v25, v25\n"
    "v_fma_f32 v22, v22, s88, v21\n"
    "v_fma_f32 v23, v23, s88, v21\n"
    "v_mul_f32 v24, v70, v24\n"
    "v_mul_f32 v25, v71, v25\n"
    "v_cvt_pk_bf16_f32 v35, v24, v25\n"
    "v_mul_f32 v22, v72, v22\n"
    "v_mul_f32 v23, v73, v23\n"
    "v_exp_f32 v22, v22\n"
    "v_exp_f32 v23, v23\n"
    "v_mul_f32 v24, v74, v74\n"
    "v_mul_f32 v25, v75, v75\n"
    "v_add_f32 v22, 1.0, v22\n"
    "v_add_f32 v23, 1.0, v23\n"
    "s_add_u32 s69, s37, 4096\n"
    "buffer_load_dwordx4 v[120:123], v0, s[16:19], s69 offen offset:0 sc1\n"
    "v_rcp_f32 v22, v22\n"
    "v_rcp_f32 v23, v23\n"
    "v_fma_f32 v24, v24, s88, v21\n"
    "v_fma_f32 v25, v25, s88, v21\n"
    "v_mul_f32 v22, v72, v22\n"
    "v_mul_f32 v23, v73, v23\n"
    "v_cvt_pk_bf16_f32 v36, v22, v23\n"
    "v_mul_f32 v24, v74, v24\n"
    "v_mul_f32 v25, v75, v25\n"
    "v_exp_f32 v24, v24\n"
    "v_exp_f32 v25, v25\n"
    "v_mul_f32 v22, v76, v76\n"
    "v_mul_f32 v23, v77, v77\n"
    "v_add_f32 v24, 1.0, v24\n"
    "v_add_f32 v25, 1.0, v25\n"
    "v_rcp_f32 v24, v24\n"
    "v_rcp_f32 v25, v25\n"
    "v_fma_f32 v22, v22, s88, v21\n"
    "v_fma_f32 v23, v23, s88, v21\n"
    "v_mul_f32 v24, v74, v24\n"
    "v_mul_f32 v25, v75, v25\n"
    "v_cvt_pk_bf16_f32 v37, v24, v25\n"
    "v_mul_f32 v22, v76, v22\n"
    "v_mul_f32 v23, v77, v23\n"
    "v_exp_f32 v22, v22\n"
    "v_exp_f32 v23, v23\n"
    "v_mul_f32 v24, v78, v78\n"
    "v_mul_f32 v25, v79, v79\n"
    "v_add_f32 v22, 1.0, v22\n"
    "v_add_f32 v23, 1.0, v23\n"
    "v_rcp_f32 v22, v22\n"
    "v_rcp_f32 v23, v23\n"
    "v_fma_f32 v24, v24, s88, v21\n"
    "v_fma_f32 v25, v25, s88, v21\n"
    "v_mul_f32 v22, v76, v22\n"
    "v_mul_f32 v23, v77, v23\n"
    "v_cvt_pk_bf16_f32 v38, v22, v23\n"
    "v_mul_f32 v24, v78, v24\n"
    "v_mul_f32 v25, v79, v25\n"
    "v_exp_f32 v24, v24\n"
    "v_exp_f32 v25, v25\n"
    "v_add_f32 v24, 1.0, v24\n"
    "v_add_f32 v25, 1.0, v25\n"
    "v_rcp_f32 v24, v24\n"
    "v_rcp_f32 v25, v25\n"
    "v_mul_f32 v24, v78, v24\n"
    "v_mul_f32 v25, v79, v25\n"
    "v_cvt_pk_bf16_f32 v39, v24, v25\n"
    "s_add_u32 s71, s79, 43008\n"
    "buffer_store_dwordx4 v[32:35], v0, s[16:19], s71 offen offset:0 sc1\n"
    "buffer_store_dwordx4 v[36:39], v0, s[16:19], s71 offen offset:1024 sc1\n"
    "s_add_u32 s69, s36, 4096\n"
    "buffer_load_dwordx4 v[124:127], v0, s[16:19], s69 offen offset:0 sc1\n"
    "s_add_u32 s69, s37, 8192\n"
    "buffer_load_dwordx4 v[128:131], v0, s[16:19], s69 offen offset:0 sc1\n"
    "s_add_u32 s69, s36, 8192\n"
    "buffer_load_dwordx4 v[132:135], v0, s[16:19], s69 offen offset:0 sc1\n"
    "s_add_u32 m0, s85, 0\n"
    "s_add_u32 s70, s72, 57344\n"
    "buffer_load_dwordx4 v5, s[20:23], s70 offen lds\n"
    "s_add_u32 m0, s85, 1024\n"
    "s_add_u32 s70, s72, 64512\n"
    "buffer_load_dwordx4 v5, s[20:23], s70 offen lds\n"
    "s_add_u32 m0, s85, 8192\n"
    "s_add_u32 s70, s73, 57344\n"
    "buffer_load_dwordx4 v5, s[20:23], s70 offen lds\n"
    "s_add_u32 m0, s85, 9216\n"
    "s_add_u32 s70, s73, 64512\n"
    "buffer_load_dwordx4 v5, s[20:23], s70 offen lds\n"
    "s_add_u32 m0, s85, 16384\n"
    "s_add_u32 s70, s74, 57344\n"
    "buffer_load_dwordx4 v5, s[20:23], s70 offen lds\n"
    "s_add_u32 m0, s85, 17408\n"
    "s_add_u32 s70, s74, 64512\n"
    "buffer_load_dwordx4 v5, s[20:23], s70 offen lds\n"
    "v_xor_b32 v2, 64, v141\n"
    "v_add_u32 v3, 57344, v141\n"
    "v_add_u32 v4, 57344, v2\n"
    "v_lshrrev_b32 v23, 5, v30\n"
    "v_and_b32 v22, 0x70, v0\n"
    "v_add_u32 v22, 163072, v22\n"
    "s_mul_i32 s70, s58, 12288\n"
    "s_add_u32 s70, s70, 106496\n"
    "v_bfe_u32 v24, v30, 2, 1\n"
    "v_xor_b32 v24, 1, v24\n"
    "v_and_b32 v24, v24, v23\n"
    "v_lshlrev_b32 v24, 6, v24\n"
    "v_xor_b32 v13, v24, v141\n"
    "v_mul_u32_u24 v23, 384, v23\n"
    "v_add3_u32 v13, v13, v23, s70\n"
    "v_xor_b32 v15, 64, v13\n"
    "v_min_u32 v21, 150776, v13\n"
    "v_min_u32 v6, 150776, v15\n"
    "ds_read_b128 v[8:11], v22\n"
    "buffer_load_dwordx2 v[26:27], v143, s[16:19], s38 offen offset:2048 sc1\n"
    "v_lshrrev_b32 v111, 3, v30\n"
    "v_and_b32 v110, 7, v30\n"
    "v_mul_u32_u24 v109, 896, v111\n"
    "v_lshl_add_u32 v109, v110, 4, v109\n"
    "v_lshl_add_u32 v111, s58, 3, v111\n"
    "v_mul_u32_u24 v108, 1088, v110\n"
    "v_lshlrev_b32 v110, 10, v110\n"
    "v_lshl_add_u32 v108, v111, 4, v108\n"
    "v_lshl_add_u32 v110, v111, 4, v110\n"
    "v_lshlrev_b32 v111, 2, v111\n"
    "s_lshl2_add_u32 s71, s56, s58\n"
    "s_xor_b32 s70, s71, 0\n"
    "s_mul_i32 s70, s70, 2176\n"
    "v_add_u32 v104, s70, v0\n"
    "s_xor_b32 s70, s71, 1\n"
    "s_mul_i32 s70, s70, 2176\n"
    "v_add_u32 v105, s70, v0\n"
    "s_xor_b32 s70, s71, 2\n"
    "s_mul_i32 s70, s70, 2176\n"
    "v_add_u32 v106, s70, v0\n"
    "s_xor_b32 s70, s71, 3\n"
    "s_mul_i32 s70, s70, 2176\n"
    "v_add_u32 v107, s70, v0\n"
    "s_add_u32 m0, s85, 32768\n"
    "s_add_u32 s70, s76, 57344\n"
    "buffer_load_dwordx4 v5, s[20:23], s70 offen lds\n"
    "s_waitcnt vmcnt(11)\n"
    "s_barrier\n"
    "ds_read_b64_tr_b16 v[80:81], v3 offset:24576\n"
    "ds_read_b64_tr_b16 v[82:83], v3 offset:25600\n"
    "ds_read_b64_tr_b16 v[84:85], v4 offset:24576\n"
    "ds_read_b64_tr_b16 v[86:87], v4 offset:25600\n"
    "ds_read_b64_tr_b16 v[88:89], v3 offset:32768\n"
    "ds_read_b64_tr_b16 v[90:91], v3 offset:33792\n"
    "ds_read_b64_tr_b16 v[92:93], v4 offset:32768\n"
    "ds_read_b64_tr_b16 v[94:95], v4 offset:33792\n"
    "s_add_u32 m0, s93, 151552\n"
    "s_add_u32 s70, s45, 315392\n"
    "buffer_load_dwordx4 v5, s[24:27], s70 offen lds\n"
    "s_add_u32 s71, s80, 4096\n"
    "buffer_store_dwordx4 v[16:19], v142, s[16:19], s71 offen offset:0 sc1\n"
    "ds_read_b64_tr_b16 v[96:97], v3 offset:40960\n"
    "ds_read_b64_tr_b16 v[98:99], v3 offset:41984\n"
    "ds_read_b64_tr_b16 v[100:101], v4 offset:40960\n"
    "ds_read_b64_tr_b16 v[102:103], v4 offset:41984\n"
    "s_waitcnt vmcnt(18) lgkmcnt(8)\n"
    "v_mfma_f32_32x32x16_bf16 v[176:191], v[80:83], v[112:115], 0\n"
    "v_mfma_f32_32x32x16_bf16 v[192:207], v[84:87], v[112:115], 0\n"
    "s_add_u32 m0, s85, 33792\n"
    "s_add_u32 s70, s76, 64512\n"
    "buffer_load_dwordx4 v5, s[20:23], s70 offen lds\n"
    "s_waitcnt vmcnt(18)\n"
    "v_mfma_f32_32x32x16_bf16 v[32:47], v[80:83], v[116:119], 0\n"
    "v_mfma_f32_32x32x16_bf16 v[48:63], v[84:87], v[116:119], 0\n"
    "s_add_u32 m0, s93, 147456\n"
    "s_add_u32 s70, s45, 286720\n"
    "buffer_load_dwordx4 v5, s[24:27], s70 offen lds\n"
    "ds_read_b64_tr_b16 v[80:81], v3 offset:49152\n"
    "ds_read_b64_tr_b16 v[82:83], v3 offset:50176\n"
    "ds_read_b64_tr_b16 v[84:85], v4 offset:49152\n"
    "ds_read_b64_tr_b16 v[86:87], v4 offset:50176\n"
    "s_waitcnt vmcnt(17) lgkmcnt(8)\n"
    "v_mfma_f32_32x32x16_bf16 v[176:191], v[88:91], v[120:123], v[176:191]\n"
    "v_mfma_f32_32x32x16_bf16 v[192:207], v[92:95], v[120:123], v[192:207]\n"
    "s_waitcnt vmcnt(14)\n"
    "v_mfma_f32_32x32x16_bf16 v[32:47], v[88:91], v[124:127], v[32:47]\n"
    "v_mfma_f32_32x32x16_bf16 v[48:63], v[92:95], v[124:127], v[48:63]\n"
    "s_add_u32 m0, s85, 50176\n"
    "s_add_u32 s70, s78, 64512\n"
    "buffer_load_dwordx4 v5, s[20:23], s70 offen lds\n"
    "s_waitcnt vmcnt(6)\n"
    "v_cmp_eq_u64 vcc, s[52:53], v[26:27]\n"
    "s_nop 0\n"
    "s_andn2_b32 s70, 127, vcc_lo\n"
    "s_cbranch_scc1 .Lspin1_%=\n"
    ".Lok1_%=:\n"
    "s_add_u32 s38, s38, 4096\n"
    "ds_read_b64_tr_b16 v[88:89], v3 offset:0\n"
    "ds_read_b64_tr_b16 v[90:91], v3 offset:1024\n"
    "ds_read_b64_tr_b16 v[92:93], v4 offset:0\n"
    "ds_read_b64_tr_b16 v[94:95], v4 offset:1024\n"
    "s_waitcnt vmcnt(14) lgkmcnt(8)\n"
    "v_mfma_f32_32x32x16_bf16 v[176:191], v[96:99], v[128:131], v[176:191]\n"
    "v_mfma_f32_32x32x16_bf16 v[192:207], v[100:103], v[128:131], v[192:207]\n"
    "s_add_u32 s69, s37, 12288\n"
    "buffer_load_dwordx4 v[136:139], v0, s[16:19], s69 offen offset:0 sc1\n"
    "s_add_u32 s69, s36, 12288\n"
    "buffer_load_dwordx4 v[144:147], v0, s[16:19], s69 offen offset:0 sc1\n"
    "s_waitcnt vmcnt(15)\n"
    "v_mfma_f32_32x32x16_bf16 v[32:47], v[96:99], v[132:135], v[32:47]\n"
    "v_mfma_f32_32x32x16_bf16 v[48:63], v[100:103], v[132:135], v[48:63]\n"
    "s_add_u32 s69, s36, 20480\n"
    "buffer_load_dwordx4 v[160:163], v0, s[16:19], s69 offen offset:0 sc1\n"
    "s_add_u32 s69, s37, 20480\n"
    "buffer_load_dwordx4 v[156:159], v0, s[16:19], s69 offen offset:0 sc1\n"
    "buffer_load_dwordx4 v[208:211], v0, s[16:19], s41 offen offset:0 sc1\n"
    "buffer_load_dwordx4 v[212:215], v0, s[16:19], s41 offen offset:1024 sc1\n"
    "s_add_u32 s69, s37, 16384\n"
    "buffer_load_dwordx4 v[148:151], v0, s[16:19], s69 offen offset:0 sc1\n"
    "s_add_u32 s69, s36, 16384\n"
    "buffer_load_dwordx4 v[152:155], v0, s[16:19], s69 offen offset:0 sc1\n"
    "s_add_u32 s69, s36, 24576\n"
    "buffer_load_dwordx4 v[168:171], v0, s[16:19], s69 offen offset:0 sc1\n"
    "ds_read_b64_tr_b16 v[96:97], v3 offset:8192\n"
    "ds_read_b64_tr_b16 v[98:99], v3 offset:9216\n"
    "ds_read_b64_tr_b16 v[100:101], v4 offset:8192\n"
    "ds_read_b64_tr_b16 v[102:103], v4 offset:9216\n"
    "s_waitcnt vmcnt(8) lgkmcnt(8)\n"
    "v_mfma_f32_32x32x16_bf16 v[176:191], v[80:83], v[136:139], v[176:191]\n"
    "v_mfma_f32_32x32x16_bf16 v[192:207], v[84:87], v[136:139], v[192:207]\n"
    "s_add_u32 s69, s37, 24576\n"
    "buffer_load_dwordx4 v[164:167], v0, s[16:19], s69 offen offset:0 sc1\n"
    "s_waitcnt vmcnt(8)\n"
    "v_mfma_f32_32x32x16_bf16 v[32:47], v[80:83], v[144:147], v[32:47]\n"
    "v_mfma_f32_32x32x16_bf16 v[48:63], v[84:87], v[144:147], v[48:63]\n"
    "s_cmp_eq_u32 s58, 3\n"
    "s_cbranch_scc0 .Lw4_%=\n"
    "buffer_load_dwordx2 v[26:27], v143, s[16:19], s38 offen offset:0 sc1\n"
    ".Lw4_%=:\n"
    "ds_read_b64_tr_b16 v[80:81], v3 offset:16384\n"
    "ds_read_b64_tr_b16 v[82:83], v3 offset:17408\n"
    "ds_read_b64_tr_b16 v[84:85], v4 offset:16384\n"
    "ds_read_b64_tr_b16 v[86:87], v4 offset:17408\n"
    "s_waitcnt vmcnt(3) lgkmcnt(8)\n"
    "v_mfma_f32_32x32x16_bf16 v[176:191], v[88:91], v[148:151], v[176:191]\n"
    "v_mfma_f32_32x32x16_bf16 v[192:207], v[92:95], v[148:151], v[192:207]\n"
    "s_waitcnt vmcnt(2)\n"
    "v_mfma_f32_32x32x16_bf16 v[32:47], v[88:91], v[152:155], v[32:47]\n"
    "v_mfma_f32_32x32x16_bf16 v[48:63], v[92:95], v[152:155], v[48:63]\n"
    "s_add_u32 s70, s93, 155648\n"
    "v_add_u32 v22, s70, v0\n"
    "s_waitcnt vmcnt(4)\n"
    "v_xor_b32 v209, s52, v209\n"
    "v_bitop3_b32 v209, v209, v211, s52 bitop3:0xf6\n"
    "v_bitop3_b32 v209, v209, v213, s52 bitop3:0xf6\n"
    "v_bitop3_b32 v209, v209, v215, s52 bitop3:0xf6\n"
    "v_cmp_ne_u32 vcc, 0, v209\n"
    "s_cbranch_vccnz .Lspin2_%=\n"
    ".Lok2_%=:\n"
    "ds_write2_b32 v22, v208, v210 offset0:0 offset1:1\n"
    "ds_write2_b32 v22, v212, v214 offset0:2 offset1:3\n"
    "s_cmp_eq_u32 s58, 3\n"
    "s_cbranch_scc0 .Lw5_%=\n"
    "s_waitcnt vmcnt(0)\n"
    "v_cmp_eq_u64 vcc, s[52:53], v[26:27]\n"
    "s_nop 0\n"
    "s_andn2_b32 s70, 127, vcc_lo\n"
    "s_cbranch_scc1 .Lspin3_%=\n"
    ".Lok3_%=:\n"
    ".Lw5_%=:\n"
    "s_waitcnt vmcnt(10) lgkmcnt(0)\n"
    "s_barrier\n"
    "s_add_u32 s69, s37, 28672\n"
    "buffer_load_dwordx4 v[172:175], v0, s[16:19], s69 offen offset:0 sc1\n"
    "s_add_u32 s69, s37, 32768\n"
    "buffer_load_dwordx4 v[112:115], v0, s[16:19], s69 offen offset:0 sc1\n"
    "s_add_u32 s69, s37, 36864\n"
    "buffer_load_dwordx4 v[116:119], v0, s[16:19], s69 offen offset:0 sc1\n"
    "s_add_u32 s69, s37, 40960\n"
    "buffer_load_dwordx4 v[120:123], v0, s[16:19], s69 offen offset:0 sc1\n"
    "s_add_u32 s69, s37, 45056\n"
    "buffer_load_dwordx4 v[124:127], v0, s[16:19], s69 offen offset:0 sc1\n"
    "s_add_u32 s69, s37, 49152\n"
    "buffer_load_dwordx4 v[128:131], v0, s[16:19], s69 offen offset:0 sc1\n"
    "ds_read_b64_tr_b16 v[88:89], v141 offset:0\n"
    "ds_read_b64_tr_b16 v[90:91], v141 offset:1024\n"
    "ds_read_b64_tr_b16 v[92:93], v2 offset:0\n"
    "ds_read_b64_tr_b16 v[94:95], v2 offset:1024\n"
    "s_waitcnt vmcnt(12) lgkmcnt(10)\n"
    "v_mfma_f32_32x32x16_bf16 v[176:191], v[96:99], v[156:159], v[176:191]\n"
    "v_mfma_f32_32x32x16_bf16 v[192:207], v[100:103], v[156:159], v[192:207]\n"
    "s_waitcnt vmcnt(13)\n"
    "v_mfma_f32_32x32x16_bf16 v[32:47], v[96:99], v[160:163], v[32:47]\n"
    "v_mfma_f32_32x32x16_bf16 v[48:63], v[100:103], v[160:163], v[48:63]\n"
    "s_add_u32 m0, s93, 110592\n"
    "s_add_u32 s70, s45, 28672\n"
    "buffer_load_dwordx4 v5, s[24:27], s70 offen lds\n"
    "s_add_u32 s69, s37, 53248\n"
    "buffer_load_dwordx4 v[132:135], v0, s[16:19], s69 offen offset:0 sc1\n"
    "s_add_u32 m0, s93, 106496\n"
    "s_add_u32 s70, s45, 0\n"
    "buffer_load_dwordx4 v5, s[24:27], s70 offen lds\n"
    "ds_read_b64_tr_b16 v[96:97], v141 offset:8192\n"
    "ds_read_b64_tr_b16 v[98:99], v141 offset:9216\n"
    "ds_read_b64_tr_b16 v[100:101], v2 offset:8192\n"
    "ds_read_b64_tr_b16 v[102:103], v2 offset:9216\n"
    "s_waitcnt vmcnt(9) lgkmcnt(10)\n"
    "v_mfma_f32_32x32x16_bf16 v[176:191], v[80:83], v[164:167], v[176:191]\n"
    "v_mfma_f32_32x32x16_bf16 v[192:207], v[84:87], v[164:167], v[192:207]\n"
    "s_add_u32 s69, s36, 28672\n"
    "buffer_load_dwordx4 v[136:139], v0, s[16:19], s69 offen offset:0 sc1\n"
    "s_add_u32 s69, s36, 32768\n"
    "buffer_load_dwordx4 v[144:147], v0, s[16:19], s69 offen offset:0 sc1\n"
    "s_waitcnt vmcnt(12)\n"
    "v_mfma_f32_32x32x16_bf16 v[32:47], v[80:83], v[168:171], v[32:47]\n"
    "v_mfma_f32_32x32x16_bf16 v[48:63], v[84:87], v[168:171], v[48:63]\n"
    "s_add_u32 s69, s36, 40960\n"
    "buffer_load_dwordx4 v[152:155], v0, s[16:19], s69 offen offset:0 sc1\n"
    "ds_read_b64_tr_b16 v[80:81], v141 offset:16384\n"
    "ds_read_b64_tr_b16 v[82:83], v141 offset:17408\n"
    "ds_read_b64_tr_b16 v[84:85], v2 offset:16384\n"
    "ds_read_b64_tr_b16 v[86:87], v2 offset:17408\n"
    "s_waitcnt vmcnt(11) lgkmcnt(8)\n"
    "v_mfma_f32_32x32x16_bf16 v[176:191], v[88:91], v[172:175], v[176:191]\n"
    "v_mfma_f32_32x32x16_bf16 v[192:207], v[92:95], v[172:175], v[192:207]\n"
    "s_add_u32 s69, s36, 36864\n"
    "buffer_load_dwordx4 v[148:151], v0, s[16:19], s69 offen offset:0 sc1\n"
    "s_add_u32 s69, s36, 45056\n"
    "buffer_load_dwordx4 v[156:159], v0, s[16:19], s69 offen offset:0 sc1\n"
    "s_add_u32 s69, s36, 49152\n"
    "buffer_load_dwordx4 v[160:163], v0, s[16:19], s69 offen offset:0 sc1\n"
    "ds_read_b64_tr_b16 v[88:89], v141 offset:24576\n"
    "ds_read_b64_tr_b16 v[90:91], v141 offset:25600\n"
    "ds_read_b64_tr_b16 v[92:93], v2 offset:24576\n"
    "ds_read_b64_tr_b16 v[94:95], v2 offset:25600\n"
    "s_waitcnt vmcnt(13) lgkmcnt(8)\n"
    "v_mfma_f32_32x32x16_bf16 v[176:191], v[96:99], v[112:115], v[176:191]\n"
    "v_mfma_f32_32x32x16_bf16 v[192:207], v[100:103], v[112:115], v[192:207]\n"
    "ds_read_b64_tr_b16 v[96:97], v141 offset:32768\n"
    "ds_read_b64_tr_b16 v[98:99], v141 offset:33792\n"
    "ds_read_b64_tr_b16 v[100:101], v2 offset:32768\n"
    "ds_read_b64_tr_b16 v[102:103], v2 offset:33792\n"
    "s_waitcnt vmcnt(12) lgkmcnt(8)\n"
    "v_mfma_f32_32x32x16_bf16 v[176:191], v[80:83], v[116:119], v[176:191]\n"
    "v_mfma_f32_32x32x16_bf16 v[192:207], v[84:87], v[116:119], v[192:207]\n"
    "s_add_u32 s69, s36, 53248\n"
    "buffer_load_dwordx4 v[164:167], v0, s[16:19], s69 offen offset:0 sc1\n"
    "ds_read_b64_tr_b16 v[80:81], v141 offset:40960\n"
    "ds_read_b64_tr_b16 v[82:83], v141 offset:41984\n"
    "ds_read_b64_tr_b16 v[84:85], v2 offset:40960\n"
    "ds_read_b64_tr_b16 v[86:87], v2 offset:41984\n"
    "s_waitcnt vmcnt(12) lgkmcnt(8)\n"
    "v_mfma_f32_32x32x16_bf16 v[176:191], v[88:91], v[120:123], v[176:191]\n"
    "v_mfma_f32_32x32x16_bf16 v[192:207], v[92:95], v[120:123], v[192:207]\n"
    "ds_read_b64_tr_b16 v[88:89], v141 offset:49152\n"
    "ds_read_b64_tr_b16 v[90:91], v141 offset:50176\n"
    "ds_read_b64_tr_b16 v[92:93], v2 offset:49152\n"
    "ds_read_b64_tr_b16 v[94:95], v2 offset:50176\n"
    "s_waitcnt vmcnt(11) lgkmcnt(8)\n"
    "v_mfma_f32_32x32x16_bf16 v[176:191], v[96:99], v[124:127], v[176:191]\n"
    "v_mfma_f32_32x32x16_bf16 v[192:207], v[100:103], v[124:127], v[192:207]\n"
    "ds_read_b64_tr_b16 v[96:97], v141 offset:0\n"
    "ds_read_b64_tr_b16 v[98:99], v141 offset:1024\n"
    "ds_read_b64_tr_b16 v[100:101], v2 offset:0\n"
    "ds_read_b64_tr_b16 v[102:103], v2 offset:1024\n"
    "s_waitcnt vmcnt(10) lgkmcnt(8)\n"
    "v_mfma_f32_32x32x16_bf16 v[176:191], v[80:83], v[128:131], v[176:191]\n"
    "v_mfma_f32_32x32x16_bf16 v[192:207], v[84:87], v[128:131], v[192:207]\n"
    "ds_read_b64_tr_b16 v[80:81], v141 offset:8192\n"
    "ds_read_b64_tr_b16 v[82:83], v141 offset:9216\n"
    "ds_read_b64_tr_b16 v[84:85], v2 offset:8192\n"
    "ds_read_b64_tr_b16 v[86:87], v2 offset:9216\n"
    "s_waitcnt vmcnt(8) lgkmcnt(8)\n"
    "v_mfma_f32_32x32x16_bf16 v[176:191], v[88:91], v[132:135], v[176:191]\n"
    "v_mfma_f32_32x32x16_bf16 v[192:207], v[92:95], v[132:135], v[192:207]\n"
    "s_add_u32 s70, s85, 57344\n"
    "v_add_u32 v1, s70, v0\n"
    "v_xor_b32 v22, 0x800, v1\n"
    "v_xor_b32 v23, 0x1000, v1\n"
    "v_xor_b32 v24, 0x1800, v1\n"
    "s_nop 5\n"
    "ds_write_b128 v22, v[184:187] offset:0\n"
    "ds_write_b128 v22, v[188:191] offset:1024\n"
    "ds_write_b128 v23, v[192:195] offset:8192\n"
    "ds_write_b128 v23, v[196:199] offset:9216\n"
    "ds_write_b128 v24, v[200:203] offset:16384\n"
    "ds_write_b128 v24, v[204:207] offset:17408\n"
    "s_setprio 0\n"
    "s_waitcnt lgkmcnt(0)\n"
    "s_barrier\n"
    "ds_read_b128 v[184:187], v1 offset:0\n"
    "ds_read_b128 v[188:191], v1 offset:1024\n"
    "ds_read_b128 v[192:195], v1 offset:8192\n"
    "ds_read_b128 v[196:199], v1 offset:9216\n"
    "ds_read_b128 v[200:203], v1 offset:16384\n"
    "ds_read_b128 v[204:207], v1 offset:17408\n"
    "s_add_u32 m0, s93, 155648\n"
    "s_add_u32 s70, s45, 344064\n"
    "buffer_load_dwordx4 v5, s[24:27], s70 offen lds\n"
    "ds_read_b64_tr_b16 v[88:89], v141 offset:16384\n"
    "ds_read_b64_tr_b16 v[90:91], v141 offset:17408\n"
    "ds_read_b64_tr_b16 v[92:93], v2 offset:16384\n"
    "ds_read_b64_tr_b16 v[94:95], v2 offset:17408\n"
    "s_waitcnt vmcnt(7) lgkmcnt(15)\n"
    "v_mfma_f32_32x32x16_bf16 v[32:47], v[96:99], v[136:139], v[32:47]\n"
    "v_mfma_f32_32x32x16_bf16 v[48:63], v[100:103], v[136:139], v[48:63]\n"
    "buffer_load_dwordx4 v[168:171], v0, s[16:19], s39 offen offset:0 sc1\n"
    "buffer_load_dwordx4 v[172:175], v0, s[16:19], s39 offen offset:1024 sc1\n"
    "buffer_load_dwordx4 v[112:115], v0, s[16:19], s39 offen offset:2048 sc1\n"
    "ds_read_b64_tr_b16 v[96:97], v141 offset:24576\n"
    "ds_read_b64_tr_b16 v[98:99], v141 offset:25600\n"
    "ds_read_b64_tr_b16 v[100:101], v2 offset:24576\n"
    "ds_read_b64_tr_b16 v[102:103], v2 offset:25600\n"
    "s_waitcnt vmcnt(9) lgkmcnt(15)\n"
    "v_mfma_f32_32x32x16_bf16 v[32:47], v[80:83], v[144:147], v[32:47]\n"
    "v_mfma_f32_32x32x16_bf16 v[48:63], v[84:87], v[144:147], v[48:63]\n"
    "s_add_u32 m0, s93, 159744\n"
    "s_add_u32 s70, s45, 372736\n"
    "buffer_load_dwordx4 v5, s[24:27], s70 offen lds\n"
    "ds_read_b64_tr_b16 v[80:81], v141 offset:32768\n"
    "ds_read_b64_tr_b16 v[82:83], v141 offset:33792\n"
    "ds_read_b64_tr_b16 v[84:85], v2 offset:32768\n"
    "ds_read_b64_tr_b16 v[86:87], v2 offset:33792\n"
    "s_waitcnt vmcnt(8) lgkmcnt(8)\n"
    "v_mfma_f32_32x32x16_bf16 v[32:47], v[88:91], v[148:151], v[32:47]\n"
    "v_mfma_f32_32x32x16_bf16 v[48:63], v[92:95], v[148:151], v[48:63]\n"
    "s_waitcnt lgkmcnt(12)\n"
    "v_mov_b32 v25, 0xff\n"
    "v_add_f32 v176, v176, v184\n"
    "v_add_f32 v176, v176, v192\n"
    "v_add_f32 v176, v176, v200\n"
    "v_bfi_b32 v176, v25, s12, v176\n"
    "v_add_f32 v177, v177, v185\n"
    "v_add_f32 v177, v177, v193\n"
    "v_add_f32 v177, v177, v201\n"
    "v_bfi_b32 v177, v25, s13, v177\n"
    "v_add_f32 v178, v178, v186\n"
    "v_add_f32 v178, v178, v194\n"
    "v_add_f32 v178, v178, v202\n"
    "v_bfi_b32 v178, v25, s14, v178\n"
    "v_add_f32 v179, v179, v187\n"
    "v_add_f32 v179, v179, v195\n"
    "v_add_f32 v179, v179, v203\n"
    "v_bfi_b32 v179, v25, s15, v179\n"
    "buffer_store_dwordx4 v[176:179], v0, s[16:19], s43 offen offset:0 sc1\n"
    "v_add_f32 v180, v180, v188\n"
    "v_add_f32 v180, v180, v196\n"
    "v_add_f32 v180, v180, v204\n"
    "v_bfi_b32 v180, v25, s12, v180\n"
    "v_add_f32 v181, v181, v189\n"
    "v_add_f32 v181, v181, v197\n"
    "v_add_f32 v181, v181, v205\n"
    "v_bfi_b32 v181, v25, s13, v181\n"
    "v_add_f32 v182, v182, v190\n"
    "v_add_f32 v182, v182, v198\n"
    "v_add_f32 v182, v182, v206\n"
    "v_bfi_b32 v182, v25, s14, v182\n"
    "v_add_f32 v183, v183, v191\n"
    "v_add_f32 v183, v183, v199\n"
    "v_add_f32 v183, v183, v207\n"
    "v_bfi_b32 v183, v25, s15, v183\n"
    "buffer_store_dwordx4 v[180:183], v0, s[16:19], s43 offen offset:1024 sc1\n"
    "v_lshlrev_b32 v176, 16, v8\n"
    "v_and_b32 v177, 0xffff0000, v8\n"
    "v_lshlrev_b32 v178, 16, v9\n"
    "v_and_b32 v179, 0xffff0000, v9\n"
    "v_lshlrev_b32 v180, 16, v10\n"
    "v_and_b32 v181, 0xffff0000, v10\n"
    "v_lshlrev_b32 v182, 16, v11\n"
    "v_and_b32 v183, 0xffff0000, v11\n"
    "buffer_load_dwordx4 v[116:119], v0, s[16:19], s39 offen offset:3072 sc1\n"
    "ds_read_b64_tr_b16 v[88:89], v141 offset:40960\n"
    "ds_read_b64_tr_b16 v[90:91], v141 offset:41984\n"
    "ds_read_b64_tr_b16 v[92:93], v2 offset:40960\n"
    "ds_read_b64_tr_b16 v[94:95], v2 offset:41984\n"
    "s_waitcnt vmcnt(12) lgkmcnt(8)\n"
    "v_mfma_f32_32x32x16_bf16 v[32:47], v[96:99], v[152:155], v[32:47]\n"
    "v_mfma_f32_32x32x16_bf16 v[48:63], v[100:103], v[152:155], v[48:63]\n"
    "ds_read_b64_tr_b16 v[96:97], v141 offset:49152\n"
    "ds_read_b64_tr_b16 v[98:99], v141 offset:50176\n"
    "ds_read_b64_tr_b16 v[100:101], v2 offset:49152\n"
    "ds_read_b64_tr_b16 v[102:103], v2 offset:50176\n"
    "s_waitcnt vmcnt(10) lgkmcnt(8)\n"
    "v_mfma_f32_32x32x16_bf16 v[32:47], v[80:83], v[156:159], v[32:47]\n"
    "v_mfma_f32_32x32x16_bf16 v[48:63], v[84:87], v[156:159], v[48:63]\n"
    "s_waitcnt vmcnt(3) lgkmcnt(0)\n"
    "s_barrier\n"
    "s_add_u32 s69, s39, 4096\n"
    "buffer_load_dwordx4 v[120:123], v0, s[16:19], s69 offen offset:0 sc1\n"
    "buffer_load_dwordx4 v[132:135], v0, s[16:19], s69 offen offset:3072 sc1\n"
    "ds_read_b64_tr_b16 v[80:81], v13 offset:0\n"
    "ds_read_b64_tr_b16 v[82:83], v13 offset:512\n"
    "ds_read_b64_tr_b16 v[84:85], v15 offset:0\n"
    "ds_read_b64_tr_b16 v[86:87], v15 offset:512\n"
    "s_waitcnt vmcnt(11) lgkmcnt(8)\n"
    "v_mfma_f32_32x32x16_bf16 v[32:47], v[88:91], v[160:163], v[32:47]\n"
    "v_mfma_f32_32x32x16_bf16 v[48:63], v[92:95], v[160:163], v[48:63]\n"
    "ds_read_b64_tr_b16 v[88:89], v15 offset:1792\n"
    "ds_read_b64_tr_b16 v[90:91], v15 offset:2304\n"
    "ds_read_b64_tr_b16 v[92:93], v13 offset:1792\n"
    "ds_read_b64_tr_b16 v[94:95], v13 offset:2304\n"
    "s_waitcnt vmcnt(10) lgkmcnt(8)\n"
    "v_mfma_f32_32x32x16_bf16 v[32:47], v[96:99], v[164:167], v[32:47]\n"
    "v_mfma_f32_32x32x16_bf16 v[48:63], v[100:103], v[164:167], v[48:63]\n"
    "buffer_load_dwordx4 v[124:127], v0, s[16:19], s69 offen offset:1024 sc1\n"
    "buffer_load_dwordx4 v[128:131], v0, s[16:19], s69 offen offset:2048 sc1\n"
    "ds_read_b64_tr_b16 v[96:97], v13 offset:3584\n"
    "ds_read_b64_tr_b16 v[98:99], v13 offset:4096\n"
    "ds_read_b64_tr_b16 v[100:101], v15 offset:3584\n"
    "ds_read_b64_tr_b16 v[102:103], v15 offset:4096\n"
    "s_waitcnt vmcnt(10) lgkmcnt(8)\n"
    "v_xor_b32 v171, s47, v171\n"
    "v_cmp_lt_u32 vcc, 0xffff, v171\n"
    "s_cbranch_vccnz .Lspin4_%=\n"
    ".Lok4_%=:\n"
    "v_mfma_f32_32x32x16_bf16 v[32:47], v[80:83], v[168:171], v[32:47]\n"
    "v_mfma_f32_32x32x16_bf16 v[48:63], v[84:87], v[168:171], v[48:63]\n"
    "ds_read_b64_tr_b16 v[80:81], v15 offset:5376\n"
    "ds_read_b64_tr_b16 v[82:83], v15 offset:5888\n"
    "ds_read_b64_tr_b16 v[84:85], v13 offset:5376\n"
    "ds_read_b64_tr_b16 v[86:87], v13 offset:5888\n"
    "s_waitcnt vmcnt(9) lgkmcnt(8)\n"
    "v_xor_b32 v175, s47, v175\n"
    "v_cmp_lt_u32 vcc, 0xffff, v175\n"
    "s_cbranch_vccnz .Lspin5_%=\n"
    ".Lok5_%=:\n"
    "v_mfma_f32_32x32x16_bf16 v[32:47], v[88:91], v[172:175], v[32:47]\n"
    "v_mfma_f32_32x32x16_bf16 v[48:63], v[92:95], v[172:175], v[48:63]\n"
    "ds_read_b64_tr_b16 v[88:89], v13 offset:7168\n"
    "ds_read_b64_tr_b16 v[90:91], v13 offset:7680\n"
    "ds_read_b64_tr_b16 v[92:93], v15 offset:7168\n"
    "ds_read_b64_tr_b16 v[94:95], v15 offset:7680\n"
    "s_waitcnt vmcnt(8) lgkmcnt(8)\n"
    "v_xor_b32 v115, s47, v115\n"
    "v_cmp_lt_u32 vcc, 0xffff, v115\n"
    "s_cbranch_vccnz .Lspin6_%=\n"
    ".Lok6_%=:\n"
    "v_mfma_f32_32x32x16_bf16 v[32:47], v[96:99], v[112:115], v[32:47]\n"
    "v_mfma_f32_32x32x16_bf16 v[48:63], v[100:103], v[112:115], v[48:63]\n"
    "ds_read_b64_tr_b16 v[96:97], v15 offset:8960\n"
    "ds_read_b64_tr_b16 v[98:99], v15 offset:9472\n"
    "ds_read_b64_tr_b16 v[100:101], v13 offset:8960\n"
    "ds_read_b64_tr_b16 v[102:103], v13 offset:9472\n"
    "s_waitcnt vmcnt(4) lgkmcnt(8)\n"
    "v_xor_b32 v119, s47, v119\n"
    "v_cmp_lt_u32 vcc, 0xffff, v119\n"
    "s_cbranch_vccnz .Lspin7_%=\n"
    ".Lok7_%=:\n"
    "v_mfma_f32_32x32x16_bf16 v[32:47], v[80:83], v[116:119], v[32:47]\n"
    "v_mfma_f32_32x32x16_bf16 v[48:63], v[84:87], v[116:119], v[48:63]\n"
    "buffer_load_dword v12, v111, s[16:19], s40 offen sc1\n"
    "buffer_load_dwordx4 v[184:187], v110, s[16:19], s44 offen offset:0 sc1\n"
    "buffer_load_dwordx4 v[188:191], v110, s[16:19], s44 offen offset:512 sc1\n"
    "ds_read_b64_tr_b16 v[80:81], v13 offset:10752\n"
    "ds_read_b64_tr_b16 v[82:83], v13 offset:11264\n"
    "ds_read_b64_tr_b16 v[84:85], v15 offset:10752\n"
    "ds_read_b64_tr_b16 v[86:87], v15 offset:11264\n"
    "s_waitcnt vmcnt(6) lgkmcnt(8)\n"
    "v_xor_b32 v123, s47, v123\n"
    "v_cmp_lt_u32 vcc, 0xffff, v123\n"
    "s_cbranch_vccnz .Lspin8_%=\n"
    ".Lok8_%=:\n"
    "v_mfma_f32_32x32x16_bf16 v[32:47], v[88:91], v[120:123], v[32:47]\n"
    "v_mfma_f32_32x32x16_bf16 v[48:63], v[92:95], v[120:123], v[48:63]\n"
    "ds_read_b64_tr_b16 v[88:89], v15 offset:12544\n"
    "ds_read_b64_tr_b16 v[90:91], v6 offset:13056\n"
    "ds_read_b64_tr_b16 v[92:93], v13 offset:12544\n"
    "ds_read_b64_tr_b16 v[94:95], v21 offset:13056\n"
    "s_waitcnt vmcnt(4) lgkmcnt(8)\n"
    "v_xor_b32 v127, s47, v127\n"
    "v_cmp_lt_u32 vcc, 0xffff, v127\n"
    "s_cbranch_vccnz .Lspin9_%=\n"
    ".Lok9_%=:\n"
    "v_mfma_f32_32x32x16_bf16 v[32:47], v[96:99], v[124:127], v[32:47]\n"
    "v_mfma_f32_32x32x16_bf16 v[48:63], v[100:103], v[124:127], v[48:63]\n"
    "s_waitcnt vmcnt(3) lgkmcnt(4)\n"
    "v_xor_b32 v131, s47, v131\n"
    "v_cmp_lt_u32 vcc, 0xffff, v131\n"
    "s_cbranch_vccnz .Lspin10_%=\n"
    ".Lok10_%=:\n"
    "v_mfma_f32_32x32x16_bf16 v[32:47], v[80:83], v[128:131], v[32:47]\n"
    "v_mfma_f32_32x32x16_bf16 v[48:63], v[84:87], v[128:131], v[48:63]\n"
    "s_waitcnt vmcnt(5) lgkmcnt(0)\n"
    "v_xor_b32 v135, s47, v135\n"
    "v_cmp_lt_u32 vcc, 0xffff, v135\n"
    "s_cbranch_vccnz .Lspin11_%=\n"
    ".Lok11_%=:\n"
    "v_mfma_f32_32x32x16_bf16 v[32:47], v[88:91], v[132:135], v[32:47]\n"
    "v_mfma_f32_32x32x16_bf16 v[48:63], v[92:95], v[132:135], v[48:63]\n"
    "s_branch .Lfin_%=\n"
    ".Lch1_%=:\n"
    "s_sleep 5\n"
    "s_nop 6\n"
    "v_mul_f32 v22, v32, v32\n"
    "v_mul_f32 v23, v33, v33\n"
    "v_fma_f32 v22, v22, s88, v21\n"
    "v_fma_f32 v23, v23, s88, v21\n"
    "v_mul_f32 v22, v32, v22\n"
    "v_mul_f32 v23, v33, v23\n"
    "v_exp_f32 v22, v22\n"
    "v_exp_f32 v23, v23\n"
    "v_mul_f32 v24, v34, v34\n"
    "v_mul_f32 v25, v35, v35\n"
    "v_add_f32 v22, 1.0, v22\n"
    "v_add_f32 v23, 1.0, v23\n"
    "v_rcp_f32 v22, v22\n"
    "v_rcp_f32 v23, v23\n"
    "v_fma_f32 v24, v24, s88, v21\n"
    "v_fma_f32 v25, v25, s88, v21\n"
    "v_mul_f32 v22, v32, v22\n"
    "v_mul_f32 v23, v33, v23\n"
    "v_cvt_pk_bf16_f32 v112, v22, v23\n"
    "v_mul_f32 v24, v34, v24\n"
    "v_mul_f32 v25, v35, v25\n"
    "v_exp_f32 v24, v24\n"
    "v_exp_f32 v25, v25\n"
    "v_mul_f32 v22, v36, v36\n"
    "v_mul_f32 v23, v37, v37\n"
    "v_add_f32 v24, 1.0, v24\n"
    "v_add_f32 v25, 1.0, v25\n"
    "v_rcp_f32 v24, v24\n"
    "v_rcp_f32 v25, v25\n"
    "v_fma_f32 v22, v22, s88, v21\n"
    "v_fma_f32 v23, v23, s88, v21\n"
    "v_mul_f32 v24, v34, v24\n"
    "v_mul_f32 v25, v35, v25\n"
    "v_cvt_pk_bf16_f32 v113, v24, v25\n"
    "v_mul_f32 v22, v36, v22\n"
    "v_mul_f32 v23, v37, v23\n"
    "v_exp_f32 v22, v22\n"
    "v_exp_f32 v23, v23\n"
    "v_mul_f32 v24, v38, v38\n"
    "v_mul_f32 v25, v39, v39\n"
    "v_add_f32 v22, 1.0, v22\n"
    "v_add_f32 v23, 1.0, v23\n"
    "v_rcp_f32 v22, v22\n"
    "v_rcp_f32 v23, v23\n"
    "v_fma_f32 v24, v24, s88, v21\n"
    "v_fma_f32 v25, v25, s88, v21\n"
    "v_mul_f32 v22, v36, v22\n"
    "v_mul_f32 v23, v37, v23\n"
    "v_cvt_pk_bf16_f32 v114, v22, v23\n"
    "v_mul_f32 v24, v38, v24\n"
    "v_mul_f32 v25, v39, v25\n"
    "v_exp_f32 v24, v24\n"
    "v_exp_f32 v25, v25\n"
    "v_mul_f32 v22, v40, v40\n"
    "v_mul_f32 v23, v41, v41\n"
    "v_add_f32 v24, 1.0, v24\n"
    "v_add_f32 v25, 1.0, v25\n"
    "v_rcp_f32 v24, v24\n"
    "v_rcp_f32 v25, v25\n"
    "v_fma_f32 v22, v22, s88, v21\n"
    "s_add_u32 m0, s93, 24576\n"
    "s_add_u32 s94, s68, 573568\n"
    "buffer_load_dwordx4 v3, s[8:11], s94 offen lds\n"
    "v_fma_f32 v23, v23, s88, v21\n"
    "v_mul_f32 v24, v38, v24\n"
    "v_mul_f32 v25, v39, v25\n"
    "v_cvt_pk_bf16_f32 v115, v24, v25\n"
    "v_mul_f32 v22, v40, v22\n"
    "v_mul_f32 v23, v41, v23\n"
    "v_exp_f32 v22, v22\n"
    "v_exp_f32 v23, v23\n"
    "v_mul_f32 v24, v42, v42\n"
    "v_mul_f32 v25, v43, v43\n"
    "v_add_f32 v22, 1.0, v22\n"
    "v_add_f32 v23, 1.0, v23\n"
    "v_rcp_f32 v22, v22\n"
    "v_rcp_f32 v23, v23\n"
    "v_fma_f32 v24, v24, s88, v21\n"
    "v_fma_f32 v25, v25, s88, v21\n"
    "v_mul_f32 v22, v40, v22\n"
    "v_mul_f32 v23, v41, v23\n"
    "v_cvt_pk_bf16_f32 v116, v22, v23\n"
    "v_mul_f32 v24, v42, v24\n"
    "v_mul_f32 v25, v43, v25\n"
    "v_exp_f32 v24, v24\n"
    "v_exp_f32 v25, v25\n"
    "v_mul_f32 v22, v44, v44\n"
    "s_add_u32 m0, s93, 32768\n"
    "s_add_u32 s94, s68, 802944\n"
    "buffer_load_dwordx4 v3, s[8:11], s94 offen lds\n"
    "s_setprio 1\n"
    "v_mul_f32 v23, v45, v45\n"
    "v_add_f32 v24, 1.0, v24\n"
    "v_add_f32 v25, 1.0, v25\n"
    "v_rcp_f32 v24, v24\n"
    "v_rcp_f32 v25, v25\n"
    "v_fma_f32 v22, v22, s88, v21\n"
    "v_fma_f32 v23, v23, s88, v21\n"
    "v_mul_f32 v24, v42, v24\n"
    "v_mul_f32 v25, v43, v25\n"
    "v_cvt_pk_bf16_f32 v117, v24, v25\n"
    "v_mul_f32 v22, v44, v22\n"
    "v_mul_f32 v23, v45, v23\n"
    "s_add_u32 m0, s93, 36864\n"
    "s_add_u32 s94, s68, 917632\n"
    "buffer_load_dwordx4 v3, s[8:11], s94 offen lds\n"
    "v_exp_f32 v22, v22\n"
    "v_exp_f32 v23, v23\n"
    "v_mul_f32 v24, v46, v46\n"
    "v_mul_f32 v25, v47, v47\n"
    "v_add_f32 v22, 1.0, v22\n"
    "v_add_f32 v23, 1.0, v23\n"
    "v_rcp_f32 v22, v22\n"
    "v_rcp_f32 v23, v23\n"
    "v_fma_f32 v24, v24, s88, v21\n"
    "v_fma_f32 v25, v25, s88, v21\n"
    "v_mul_f32 v22, v44, v22\n"
    "v_mul_f32 v23, v45, v23\n"
    "s_add_u32 m0, s93, 40960\n"
    "s_add_u32 s94, s68, 1032320\n"
    "buffer_load_dwordx4 v3, s[8:11], s94 offen lds\n"
    "v_cvt_pk_bf16_f32 v118, v22, v23\n"
    "v_mul_f32 v24, v46, v24\n"
    "v_mul_f32 v25, v47, v25\n"
    "v_exp_f32 v24, v24\n"
    "v_exp_f32 v25, v25\n"
    "v_add_f32 v24, 1.0, v24\n"
    "v_add_f32 v25, 1.0, v25\n"
    "v_rcp_f32 v24, v24\n"
    "v_rcp_f32 v25, v25\n"
    "v_mul_f32 v24, v46, v24\n"
    "v_mul_f32 v25, v47, v25\n"
    "v_cvt_pk_bf16_f32 v119, v24, v25\n"
    "buffer_store_dwordx4 v[112:115], v4, s[16:19], s79 offen offset:0 sc1\n"
    "buffer_store_dwordx4 v[116:119], v4, s[16:19], s79 offen offset:1024 sc1\n"
    "s_waitcnt vmcnt(6)\n"
    "s_barrier\n"
    "ds_read_b64_tr_b16 v[80:81], v13 offset:0\n"
    "ds_read_b64_tr_b16 v[82:83], v13 offset:256\n"
    "ds_read_b64_tr_b16 v[84:85], v13 offset:1024\n"
    "ds_read_b64_tr_b16 v[86:87], v13 offset:1280\n"
    "ds_read_b64_tr_b16 v[88:89], v13 offset:2048\n"
    "ds_read_b64_tr_b16 v[90:91], v13 offset:2304\n"
    "ds_read_b64_tr_b16 v[92:93], v13 offset:3072\n"
    "ds_read_b64_tr_b16 v[94:95], v13 offset:3328\n"
    "ds_read_b64_tr_b16 v[96:97], v13 offset:4096\n"
    "ds_read_b64_tr_b16 v[98:99], v13 offset:4352\n"
    "s_waitcnt lgkmcnt(8)\n"
    "v_mfma_f32_32x32x16_bf16 v[48:63], v[80:83], v[144:147], 0\n"
    "s_setprio 1\n"
    "s_add_u32 m0, s93, 45056\n"
    "s_add_u32 s94, s68, 1147008\n"
    "buffer_load_dwordx4 v3, s[8:11], s94 offen lds\n"
    "ds_read_b64_tr_b16 v[100:101], v13 offset:5120\n"
    "ds_read_b64_tr_b16 v[102:103], v13 offset:5376\n"
    "s_waitcnt lgkmcnt(8)\n"
    "v_mfma_f32_32x32x16_bf16 v[48:63], v[84:87], v[148:151], v[48:63]\n"
    "ds_read_b64_tr_b16 v[80:81], v13 offset:6144\n"
    "ds_read_b64_tr_b16 v[82:83], v13 offset:6400\n"
    "s_waitcnt lgkmcnt(8)\n"
    "v_mfma_f32_32x32x16_bf16 v[48:63], v[88:91], v[152:155], v[48:63]\n"
    "ds_read_b64_tr_b16 v[84:85], v13 offset:7168\n"
    "ds_read_b64_tr_b16 v[86:87], v13 offset:7424\n"
    "s_waitcnt lgkmcnt(8)\n"
    "v_mfma_f32_32x32x16_bf16 v[48:63], v[92:95], v[156:159], v[48:63]\n"
    "s_add_u32 m0, s93, 131072\n"
    "s_add_u32 s94, s92, 0\n"
    "buffer_load_dwordx4 v0, s[16:19], s94 offen sc1 lds\n"
    "s_add_u32 m0, s93, 28672\n"
    "s_add_u32 s94, s68, 688256\n"
    "buffer_load_dwordx4 v3, s[8:11], s94 offen lds\n"
    "s_add_u32 m0, s93, 135168\n"
    "s_add_u32 s94, s92, 4096\n"
    "buffer_load_dwordx4 v0, s[16:19], s94 offen sc1 lds\n"
    "ds_read_b64_tr_b16 v[88:89], v13 offset:8192\n"
    "ds_read_b64_tr_b16 v[90:91], v13 offset:8448\n"
    "s_waitcnt lgkmcnt(8)\n"
    "v_mfma_f32_32x32x16_bf16 v[48:63], v[96:99], v[160:163], v[48:63]\n"
    "s_add_u32 m0, s93, 49152\n"
    "s_add_u32 s94, s68, 1261696\n"
    "buffer_load_dwordx4 v3, s[8:11], s94 offen lds\n"
    "ds_read_b64_tr_b16 v[92:93], v13 offset:9216\n"
    "ds_read_b64_tr_b16 v[94:95], v13 offset:9472\n"
    "s_waitcnt lgkmcnt(8)\n"
    "v_mfma_f32_32x32x16_bf16 v[48:63], v[100:103], v[164:167], v[48:63]\n"
    "s_add_u32 m0, s93, 139264\n"
    "s_add_u32 s94, s92, 8192\n"
    "buffer_load_dwordx4 v0, s[16:19], s94 offen sc1 lds\n"
    "ds_read_b64_tr_b16 v[96:97], v13 offset:10240\n"
    "ds_read_b64_tr_b16 v[98:99], v13 offset:10496\n"
    "s_waitcnt lgkmcnt(8)\n"
    "v_mfma_f32_32x32x16_bf16 v[48:63], v[80:83], v[168:171], v[48:63]\n"
    "s_add_u32 m0, s93, 53248\n"
    "s_add_u32 s94, s68, 1376384\n"
    "buffer_load_dwordx4 v3, s[8:11], s94 offen lds\n"
    "ds_read_b64_tr_b16 v[100:101], v13 offset:11264\n"
    "ds_read_b64_tr_b16 v[102:103], v13 offset:11520\n"
    "s_waitcnt lgkmcnt(8)\n"
    "v_mfma_f32_32x32x16_bf16 v[48:63], v[84:87], v[172:175], v[48:63]\n"
    "s_add_u32 m0, s93, 143360\n"
    "s_add_u32 s94, s92, 12288\n"
    "buffer_load_dwordx4 v0, s[16:19], s94 offen sc1 lds\n"
    "ds_read_b64_tr_b16 v[80:81], v13 offset:12288\n"
    "ds_read_b64_tr_b16 v[82:83], v13 offset:12544\n"
    "s_waitcnt lgkmcnt(8)\n"
    "v_mfma_f32_32x32x16_bf16 v[48:63], v[88:91], v[176:179], v[48:63]\n"
    "ds_read_b64_tr_b16 v[84:85], v13 offset:13312\n"
    "ds_read_b64_tr_b16 v[86:87], v13 offset:13568\n"
    "s_waitcnt lgkmcnt(8)\n"
    "v_mfma_f32_32x32x16_bf16 v[48:63], v[92:95], v[180:183], v[48:63]\n"
    "ds_read_b64_tr_b16 v[88:89], v13 offset:14336\n"
    "ds_read_b64_tr_b16 v[90:91], v13 offset:14592\n"
    "s_waitcnt lgkmcnt(8)\n"
    "v_mfma_f32_32x32x16_bf16 v[48:63], v[96:99], v[184:187], v[48:63]\n"
    "ds_read_b64_tr_b16 v[92:93], v13 offset:15360\n"
    "ds_read_b64_tr_b16 v[94:95], v13 offset:15616\n"
    "s_waitcnt lgkmcnt(8)\n"
    "v_mfma_f32_32x32x16_bf16 v[48:63], v[100:103], v[188:191], v[48:63]\n"
    "ds_read_b64_tr_b16 v[96:97], v13 offset:16384\n"
    "ds_read_b64_tr_b16 v[98:99], v13 offset:16640\n"
    "s_waitcnt lgkmcnt(8)\n"
    "v_mfma_f32_32x32x16_bf16 v[48:63], v[80:83], v[192:195], v[48:63]\n"
    "ds_read_b64_tr_b16 v[100:101], v13 offset:17408\n"
    "ds_read_b64_tr_b16 v[102:103], v13 offset:17664\n"
    "s_waitcnt lgkmcnt(8)\n"
    "v_mfma_f32_32x32x16_bf16 v[48:63], v[84:87], v[196:199], v[48:63]\n"
    "s_lshl2_add_u32 s89, s2, s58\n"
    "s_mul_i32 s70, s89, 13\n"
    "s_lshl_b32 s89, s89, 2\n"
    "v_cmp_gt_u32 vcc, 13, v30\n"
    "v_add_lshl_u32 v6, s70, v30, 7\n"
    "v_add_lshl_u32 v1, s89, v30, 7\n"
    "v_cndmask_b32 v6, v28, v6, vcc\n"
    "v_cmp_gt_u32 vcc, 4, v30\n"
    "buffer_load_dword v6, v6, s[20:23], 0 offen\n"
    "s_cmp_eq_u32 s64, 1\n"
    "v_cndmask_b32 v1, v28, v1, vcc\n"
    "buffer_load_dword v1, v1, s[24:27], 0 offen\n"
    "s_cselect_b32 s22, 0, s22\n"
    "s_cselect_b32 s26, 0, s26\n"
    "ds_read_b64_tr_b16 v[80:81], v13 offset:18432\n"
    "ds_read_b64_tr_b16 v[82:83], v13 offset:18688\n"
    "s_waitcnt lgkmcnt(8)\n"
    "v_mfma_f32_32x32x16_bf16 v[48:63], v[88:91], v[200:203], v[48:63]\n"
    "ds_read_b64_tr_b16 v[84:85], v13 offset:19456\n"
    "ds_read_b64_tr_b16 v[86:87], v13 offset:19712\n"
    "s_waitcnt lgkmcnt(8)\n"
    "v_mfma_f32_32x32x16_bf16 v[48:63], v[92:95], v[204:207], v[48:63]\n"
    "ds_read_b64_tr_b16 v[88:89], v13 offset:20480\n"
    "ds_read_b64_tr_b16 v[90:91], v13 offset:20736\n"
    "s_waitcnt lgkmcnt(8)\n"
    "v_mfma_f32_32x32x16_bf16 v[48:63], v[96:99], v[208:211], v[48:63]\n"
    "ds_read_b64_tr_b16 v[92:93], v13 offset:21504\n"
    "ds_read_b64_tr_b16 v[94:95], v13 offset:21760\n"
    "s_waitcnt lgkmcnt(8)\n"
    "v_mfma_f32_32x32x16_bf16 v[48:63], v[100:103], v[212:215], v[48:63]\n"
    "ds_read_b64_tr_b16 v[96:97], v13 offset:22528\n"
    "ds_read_b64_tr_b16 v[98:99], v13 offset:22784\n"
    "s_waitcnt lgkmcnt(8)\n"
    "v_mfma_f32_32x32x16_bf16 v[48:63], v[80:83], v[216:219], v[48:63]\n"
    "ds_read_b64_tr_b16 v[100:101], v13 offset:23552\n"
    "ds_read_b64_tr_b16 v[102:103], v13 offset:23808\n"
    "s_waitcnt lgkmcnt(8)\n"
    "v_mfma_f32_32x32x16_bf16 v[48:63], v[84:87], v[220:223], v[48:63]\n"
    "ds_read_b64_tr_b16 v[80:81], v13 offset:24576\n"
    "ds_read_b64_tr_b16 v[82:83], v13 offset:24832\n"
    "s_waitcnt lgkmcnt(8)\n"
    "v_mfma_f32_32x32x16_bf16 v[48:63], v[88:91], v[224:227], v[48:63]\n"
    "ds_read_b64_tr_b16 v[84:85], v13 offset:25600\n"
    "ds_read_b64_tr_b16 v[86:87], v13 offset:25856\n"
    "s_waitcnt lgkmcnt(8)\n"
    "v_mfma_f32_32x32x16_bf16 v[48:63], v[92:95], v[228:231], v[48:63]\n"
    "ds_read_b64_tr_b16 v[88:89], v13 offset:26624\n"
    "ds_read_b64_tr_b16 v[90:91], v13 offset:26880\n"
    "s_waitcnt lgkmcnt(8)\n"
    "v_mfma_f32_32x32x16_bf16 v[48:63], v[96:99], v[232:235], v[48:63]\n"
    "ds_read_b64_tr_b16 v[92:93], v13 offset:27648\n"
    "ds_read_b64_tr_b16 v[94:95], v13 offset:27904\n"
    "s_waitcnt lgkmcnt(8)\n"
    "v_mfma_f32_32x32x16_bf16 v[48:63], v[100:103], v[236:239], v[48:63]\n"
    "s_waitcnt lgkmcnt(6)\n"
    "v_mfma_f32_32x32x16_bf16 v[48:63], v[80:83], v[240:243], v[48:63]\n"
    "s_waitcnt lgkmcnt(4)\n"
    "v_mfma_f32_32x32x16_bf16 v[48:63], v[84:87], v[244:247], v[48:63]\n"
    "s_waitcnt lgkmcnt(2)\n"
    "v_mfma_f32_32x32x16_bf16 v[48:63], v[88:91], v[248:251], v[48:63]\n"
    "s_waitcnt lgkmcnt(0)\n"
    "v_mfma_f32_32x32x16_bf16 v[48:63], v[92:95], v[252:255], v[48:63]\n"
    "s_cmp_eq_u32 s64, 1\n"
    "v_add_u32 v13, 45056, v13\n"
    "s_nop 9\n"
    "v_cndmask_b32 v60, v60, 1.0, s[54:55]\n"
    "v_cndmask_b32 v62, v62, 1.0, s[54:55]\n"
    "v_cvt_pk_bf16_f32 v48, v48, v49\n"
    "v_cvt_pk_bf16_f32 v49, v50, v51\n"
    "ds_write_b64 v14, v[48:49] offset:16384\n"
    "v_cvt_pk_bf16_f32 v52, v52, v53\n"
    "v_cvt_pk_bf16_f32 v53, v54, v55\n"
    "ds_write_b64 v9, v[52:53] offset:16384\n"
    "v_cvt_pk_bf16_f32 v56, v56, v57\n"
    "v_cvt_pk_bf16_f32 v57, v58, v59\n"
    "ds_write_b64 v10, v[56:57] offset:16384\n"
    "v_cvt_pk_bf16_f32 v60, v60, v61\n"
    "v_cvt_pk_bf16_f32 v61, v62, v63\n"
    "ds_write_b64 v11, v[60:61] offset:16384\n"
    "s_cbranch_scc1 .Lsp1_%=\n"
    "s_setprio 0\n"
    "s_waitcnt vmcnt(7) lgkmcnt(0)\n"
    "s_barrier\n"
    "buffer_store_dwordx4 v[16:19], v142, s[16:19], s80 offen offset:0 sc1\n"
    "ds_read_b64_tr_b16 v[80:81], v12 offset:0\n"
    "ds_read_b64_tr_b16 v[82:83], v12 offset:512\n"
    "ds_read_b64_tr_b16 v[84:85], v12 offset:2048\n"
    "ds_read_b64_tr_b16 v[86:87], v12 offset:2560\n"
    "ds_read_b64_tr_b16 v[88:89], v12 offset:4096\n"
    "ds_read_b64_tr_b16 v[90:91], v12 offset:4608\n"
    "ds_read_b64_tr_b16 v[92:93], v12 offset:6144\n"
    "ds_read_b64_tr_b16 v[94:95], v12 offset:6656\n"
    "ds_read_b64_tr_b16 v[96:97], v12 offset:8192\n"
    "ds_read_b64_tr_b16 v[98:99], v12 offset:8704\n"
    "s_waitcnt lgkmcnt(8)\n"
    "v_mfma_f32_32x32x16_bf16 v[64:79], v[80:83], v[144:147], v[64:79]\n"
    "s_nop 4\n"
    "ds_read_b64_tr_b16 v[100:101], v12 offset:10240\n"
    "ds_read_b64_tr_b16 v[102:103], v12 offset:10752\n"
    "s_waitcnt lgkmcnt(8)\n"
    "v_mfma_f32_32x32x16_bf16 v[64:79], v[84:87], v[148:151], v[64:79]\n"
    "s_nop 4\n"
    "ds_read_b64_tr_b16 v[80:81], v12 offset:12288\n"
    "ds_read_b64_tr_b16 v[82:83], v12 offset:12800\n"
    "s_waitcnt lgkmcnt(8)\n"
    "v_mfma_f32_32x32x16_bf16 v[64:79], v[88:91], v[152:155], v[64:79]\n"
    "s_nop 4\n"
    "ds_read_b64_tr_b16 v[84:85], v12 offset:14336\n"
    "ds_read_b64_tr_b16 v[86:87], v12 offset:14848\n"
    "s_waitcnt lgkmcnt(8)\n"
    "v_mfma_f32_32x32x16_bf16 v[64:79], v[92:95], v[156:159], v[64:79]\n"
    "s_nop 4\n"
    "ds_read_b64_tr_b16 v[88:89], v12 offset:16384\n"
    "ds_read_b64_tr_b16 v[90:91], v12 offset:16896\n"
    "s_waitcnt lgkmcnt(8)\n"
    "v_mfma_f32_32x32x16_bf16 v[64:79], v[96:99], v[160:163], v[64:79]\n"
    "s_nop 4\n"
    "ds_read_b64_tr_b16 v[92:93], v12 offset:18432\n"
    "ds_read_b64_tr_b16 v[94:95], v12 offset:18944\n"
    "s_waitcnt lgkmcnt(8)\n"
    "v_mfma_f32_32x32x16_bf16 v[64:79], v[100:103], v[164:167], v[64:79]\n"
    "s_nop 4\n"
    "ds_read_b64_tr_b16 v[96:97], v12 offset:20480\n"
    "ds_read_b64_tr_b16 v[98:99], v12 offset:20992\n"
    "s_waitcnt lgkmcnt(8)\n"
    "v_mfma_f32_32x32x16_bf16 v[64:79], v[80:83], v[168:171], v[64:79]\n"
    "s_add_u32 m0, s93, 151552\n"
    "s_add_u32 s94, s92, 20480\n"
    "buffer_load_dwordx4 v0, s[16:19], s94 offen sc1 lds\n"
    "ds_read_b64_tr_b16 v[100:101], v12 offset:22528\n"
    "ds_read_b64_tr_b16 v[102:103], v12 offset:23040\n"
    "s_waitcnt lgkmcnt(8)\n"
    "v_mfma_f32_32x32x16_bf16 v[64:79], v[84:87], v[172:175], v[64:79]\n"
    "s_nop 4\n"
    "ds_read_b64_tr_b16 v[80:81], v12 offset:24576\n"
    "ds_read_b64_tr_b16 v[82:83], v12 offset:25088\n"
    "s_waitcnt lgkmcnt(8)\n"
    "v_mfma_f32_32x32x16_bf16 v[64:79], v[88:91], v[176:179], v[64:79]\n"
    "s_waitcnt vmcnt(5)\n"
    "s_barrier\n"
    "s_setprio 1\n"
    "ds_read_b64_tr_b16 v[84:85], v12 offset:26624\n"
    "ds_read_b64_tr_b16 v[86:87], v12 offset:27136\n"
    "s_waitcnt lgkmcnt(8)\n"
    "v_mfma_f32_32x32x16_bf16 v[64:79], v[92:95], v[180:183], v[64:79]\n"
    "s_nop 4\n"
    "ds_read_b64_tr_b16 v[88:89], v12 offset:28672\n"
    "ds_read_b64_tr_b16 v[90:91], v12 offset:29184\n"
    "s_waitcnt lgkmcnt(8)\n"
    "v_mfma_f32_32x32x16_bf16 v[64:79], v[96:99], v[184:187], v[64:79]\n"
    "s_add_u32 m0, s93, 147456\n"
    "s_add_u32 s94, s92, 16384\n"
    "buffer_load_dwordx4 v0, s[16:19], s94 offen sc1 lds\n"
    "ds_read_b64_tr_b16 v[92:93], v12 offset:30720\n"
    "ds_read_b64_tr_b16 v[94:95], v12 offset:31232\n"
    "s_waitcnt lgkmcnt(8)\n"
    "v_mfma_f32_32x32x16_bf16 v[64:79], v[100:103], v[188:191], v[64:79]\n"
    "s_nop 4\n"
    "ds_read_b64_tr_b16 v[96:97], v12 offset:32768\n"
    "ds_read_b64_tr_b16 v[98:99], v12 offset:33280\n"
    "s_waitcnt lgkmcnt(8)\n"
    "v_mfma_f32_32x32x16_bf16 v[64:79], v[80:83], v[192:195], v[64:79]\n"
    "s_add_u32 m0, s93, 155648\n"
    "s_add_u32 s94, s92, 24576\n"
    "buffer_load_dwordx4 v0, s[16:19], s94 offen sc1 lds\n"
    "ds_read_b64_tr_b16 v[100:101], v12 offset:34816\n"
    "ds_read_b64_tr_b16 v[102:103], v12 offset:35328\n"
    "s_waitcnt lgkmcnt(8)\n"
    "v_mfma_f32_32x32x16_bf16 v[64:79], v[84:87], v[196:199], v[64:79]\n"
    "s_nop 4\n"
    "ds_read_b64_tr_b16 v[80:81], v12 offset:36864\n"
    "ds_read_b64_tr_b16 v[82:83], v12 offset:37376\n"
    "s_waitcnt lgkmcnt(8)\n"
    "v_mfma_f32_32x32x16_bf16 v[64:79], v[88:91], v[200:203], v[64:79]\n"
    "s_nop 4\n"
    "ds_read_b64_tr_b16 v[84:85], v12 offset:38912\n"
    "ds_read_b64_tr_b16 v[86:87], v12 offset:39424\n"
    "s_waitcnt lgkmcnt(8)\n"
    "v_mfma_f32_32x32x16_bf16 v[64:79], v[92:95], v[204:207], v[64:79]\n"
    "s_nop 4\n"
    "ds_read_b64_tr_b16 v[88:89], v12 offset:40960\n"
    "ds_read_b64_tr_b16 v[90:91], v12 offset:41472\n"
    "s_waitcnt lgkmcnt(8)\n"
    "v_mfma_f32_32x32x16_bf16 v[64:79], v[96:99], v[208:211], v[64:79]\n"
    "s_nop 4\n"
    "ds_read_b64_tr_b16 v[92:93], v12 offset:43008\n"
    "ds_read_b64_tr_b16 v[94:95], v12 offset:43520\n"
    "s_waitcnt lgkmcnt(8)\n"
    "v_mfma_f32_32x32x16_bf16 v[64:79], v[100:103], v[212:215], v[64:79]\n"
    "s_nop 4\n"
    "ds_read_b64_tr_b16 v[96:97], v12 offset:45056\n"
    "ds_read_b64_tr_b16 v[98:99], v12 offset:45568\n"
    "s_waitcnt lgkmcnt(8)\n"
    "v_mfma_f32_32x32x16_bf16 v[64:79], v[80:83], v[216:219], v[64:79]\n"
    "s_nop 4\n"
    "ds_read_b64_tr_b16 v[100:101], v12 offset:47104\n"
    "ds_read_b64_tr_b16 v[102:103], v12 offset:47616\n"
    "s_waitcnt lgkmcnt(8)\n"
    "v_mfma_f32_32x32x16_bf16 v[64:79], v[84:87], v[220:223], v[64:79]\n"
    "s_nop 4\n"
    "ds_read_b64_tr_b16 v[80:81], v12 offset:49152\n"
    "ds_read_b64_tr_b16 v[82:83], v12 offset:49664\n"
    "s_waitcnt lgkmcnt(8)\n"
    "v_mfma_f32_32x32x16_bf16 v[64:79], v[88:91], v[224:227], v[64:79]\n"
    "s_nop 4\n"
    "ds_read_b64_tr_b16 v[84:85], v12 offset:51200\n"
    "ds_read_b64_tr_b16 v[86:87], v12 offset:51712\n"
    "s_waitcnt lgkmcnt(8)\n"
    "v_mfma_f32_32x32x16_bf16 v[64:79], v[92:95], v[228:231], v[64:79]\n"
    "s_nop 4\n"
    "ds_read_b64_tr_b16 v[88:89], v12 offset:53248\n"
    "ds_read_b64_tr_b16 v[90:91], v12 offset:53760\n"
    "s_waitcnt lgkmcnt(8)\n"
    "v_mfma_f32_32x32x16_bf16 v[64:79], v[96:99], v[232:235], v[64:79]\n"
    "s_nop 4\n"
    "ds_read_b64_tr_b16 v[92:93], v12 offset:55296\n"
    "ds_read_b64_tr_b16 v[94:95], v12 offset:55808\n"
    "s_waitcnt lgkmcnt(8)\n"
    "v_mfma_f32_32x32x16_bf16 v[64:79], v[100:103], v[236:239], v[64:79]\n"
    "s_nop 4\n"
    "s_waitcnt lgkmcnt(6)\n"
    "v_mfma_f32_32x32x16_bf16 v[64:79], v[80:83], v[240:243], v[64:79]\n"
    "s_nop 6\n"
    "s_waitcnt lgkmcnt(4)\n"
    "v_mfma_f32_32x32x16_bf16 v[64:79], v[84:87], v[244:247], v[64:79]\n"
    "s_setprio 0\n"
    "s_waitcnt lgkmcnt(2)\n"
    "v_mfma_f32_32x32x16_bf16 v[64:79], v[88:91], v[248:251], v[64:79]\n"
    "s_add_u32 m0, s85, 57344\n"
    "s_add_u32 s70, s76, 0\n"
    "buffer_load_dwordx4 v5, s[20:23], s70 offen lds\n"
    "s_waitcnt lgkmcnt(0)\n"
    "v_mfma_f32_32x32x16_bf16 v[64:79], v[92:95], v[252:255], v[64:79]\n"
    "s_add_u32 m0, s85, 65536\n"
    "s_add_u32 s70, s77, 0\n"
    "buffer_load_dwordx4 v5, s[20:23], s70 offen lds\n"
    "s_nop 8\n"
    "v_mul_f32 v22, v64, v64\n"
    "v_mul_f32 v23, v65, v65\n"
    "v_fma_f32 v22, v22, s88, v21\n"
    "v_fma_f32 v23, v23, s88, v21\n"
    "v_mul_f32 v22, v64, v22\n"
    "v_mul_f32 v23, v65, v23\n"
    "v_exp_f32 v22, v22\n"
    "v_exp_f32 v23, v23\n"
    "v_mul_f32 v24, v66, v66\n"
    "v_mul_f32 v25, v67, v67\n"
    "v_add_f32 v22, 1.0, v22\n"
    "v_add_f32 v23, 1.0, v23\n"
    "s_add_u32 m0, s85, 66560\n"
    "s_add_u32 s70, s77, 7168\n"
    "buffer_load_dwordx4 v5, s[20:23], s70 offen lds\n"
    "v_rcp_f32 v22, v22\n"
    "v_rcp_f32 v23, v23\n"
    "v_fma_f32 v24, v24, s88, v21\n"
    "v_fma_f32 v25, v25, s88, v21\n"
    "v_mul_f32 v22, v64, v22\n"
    "v_mul_f32 v23, v65, v23\n"
    "v_cvt_pk_bf16_f32 v32, v22, v23\n"
    "v_mul_f32 v24, v66, v24\n"
    "v_mul_f32 v25, v67, v25\n"
    "v_exp_f32 v24, v24\n"
    "v_exp_f32 v25, v25\n"
    "v_mul_f32 v22, v68, v68\n"
    "v_mul_f32 v23, v69, v69\n"
    "v_add_f32 v24, 1.0, v24\n"
    "v_add_f32 v25, 1.0, v25\n"
    "v_rcp_f32 v24, v24\n"
    "v_rcp_f32 v25, v25\n"
    "v_fma_f32 v22, v22, s88, v21\n"
    "v_fma_f32 v23, v23, s88, v21\n"
    "v_mul_f32 v24, v66, v24\n"
    "v_mul_f32 v25, v67, v25\n"
    "v_cvt_pk_bf16_f32 v33, v24, v25\n"
    "v_mul_f32 v22, v68, v22\n"
    "v_mul_f32 v23, v69, v23\n"
    "v_exp_f32 v22, v22\n"
    "v_exp_f32 v23, v23\n"
    "v_mul_f32 v24, v70, v70\n"
    "v_mul_f32 v25, v71, v71\n"
    "v_add_f32 v22, 1.0, v22\n"
    "v_add_f32 v23, 1.0, v23\n"
    "v_rcp_f32 v22, v22\n"
    "v_rcp_f32 v23, v23\n"
    "v_fma_f32 v24, v24, s88, v21\n"
    "v_fma_f32 v25, v25, s88, v21\n"
    "v_mul_f32 v22, v68, v22\n"
    "v_mul_f32 v23, v69, v23\n"
    "v_cvt_pk_bf16_f32 v34, v22, v23\n"
    "v_mul_f32 v24, v70, v24\n"
    "v_mul_f32 v25, v71, v25\n"
    "v_exp_f32 v24, v24\n"
    "v_exp_f32 v25, v25\n"
    "v_mul_f32 v22, v72, v72\n"
    "v_mul_f32 v23, v73, v73\n"
    "v_add_f32 v24, 1.0, v24\n"
    "v_add_f32 v25, 1.0, v25\n"
    "v_rcp_f32 v24, v24\n"
    "v_rcp_f32 v25, v25\n"
    "v_fma_f32 v22, v22, s88, v21\n"
    "s_add_u32 m0, s85, 74752\n"
    "s_add_u32 s70, s78, 7168\n"
    "buffer_load_dwordx4 v5, s[20:23], s70 offen lds\n"
    "v_fma_f32 v23, v23, s88, v21\n"
    "v_mul_f32 v24, v70, v24\n"
    "v_mul_f32 v25, v71, v25\n"
    "v_cvt_pk_bf16_f32 v35, v24, v25\n"
    "v_mul_f32 v22, v72, v22\n"
    "v_mul_f32 v23, v73, v23\n"
    "v_exp_f32 v22, v22\n"
    "v_exp_f32 v23, v23\n"
    "v_mul_f32 v24, v74, v74\n"
    "v_mul_f32 v25, v75, v75\n"
    "v_add_f32 v22, 1.0, v22\n"
    "v_add_f32 v23, 1.0, v23\n"
    "s_add_u32 m0, s93, 114688\n"
    "s_add_u32 s70, s45, 57344\n"
    "buffer_load_dwordx4 v5, s[24:27], s70 offen lds\n"
    "v_rcp_f32 v22, v22\n"
    "v_rcp_f32 v23, v23\n"
    "v_fma_f32 v24, v24, s88, v21\n"
    "v_fma_f32 v25, v25, s88, v21\n"
    "v_mul_f32 v22, v72, v22\n"
    "v_mul_f32 v23, v73, v23\n"
    "v_cvt_pk_bf16_f32 v36, v22, v23\n"
    "v_mul_f32 v24, v74, v24\n"
    "v_mul_f32 v25, v75, v25\n"
    "v_exp_f32 v24, v24\n"
    "v_exp_f32 v25, v25\n"
    "v_mul_f32 v22, v76, v76\n"
    "s_add_u32 m0, s93, 118784\n"
    "s_add_u32 s70, s45, 86016\n"
    "buffer_load_dwordx4 v5, s[24:27], s70 offen lds\n"
    "v_mul_f32 v23, v77, v77\n"
    "v_add_f32 v24, 1.0, v24\n"
    "v_add_f32 v25, 1.0, v25\n"
    "v_rcp_f32 v24, v24\n"
    "v_rcp_f32 v25, v25\n"
    "v_fma_f32 v22, v22, s88, v21\n"
    "v_fma_f32 v23, v23, s88, v21\n"
    "v_mul_f32 v24, v74, v24\n"
    "v_mul_f32 v25, v75, v25\n"
    "v_cvt_pk_bf16_f32 v37, v24, v25\n"
    "v_mul_f32 v22, v76, v22\n"
    "v_mul_f32 v23, v77, v23\n"
    "s_cmp_eq_u32 s58, 3\n"
    "s_cbranch_scc0 .Lw6_%=\n"
    "buffer_load_dwordx2 v[26:27], v143, s[16:19], s38 offen offset:0 sc1\n"
    ".Lw6_%=:\n"
    "s_add_u32 m0, s93, 122880\n"
    "s_add_u32 s70, s45, 114688\n"
    "buffer_load_dwordx4 v5, s[24:27], s70 offen lds\n"
    "v_exp_f32 v22, v22\n"
    "v_exp_f32 v23, v23\n"
    "v_mul_f32 v24, v78, v78\n"
    "v_mul_f32 v25, v79, v79\n"
    "v_add_f32 v22, 1.0, v22\n"
    "v_add_f32 v23, 1.0, v23\n"
    "v_rcp_f32 v22, v22\n"
    "v_rcp_f32 v23, v23\n"
    "v_fma_f32 v24, v24, s88, v21\n"
    "v_fma_f32 v25, v25, s88, v21\n"
    "v_mul_f32 v22, v76, v22\n"
    "v_mul_f32 v23, v77, v23\n"
    "s_add_u32 m0, s93, 126976\n"
    "s_add_u32 s70, s45, 143360\n"
    "buffer_load_dwordx4 v5, s[24:27], s70 offen lds\n"
    "v_cvt_pk_bf16_f32 v38, v22, v23\n"
    "v_mul_f32 v24, v78, v24\n"
    "v_mul_f32 v25, v79, v25\n"
    "v_exp_f32 v24, v24\n"
    "v_exp_f32 v25, v25\n"
    "v_add_f32 v24, 1.0, v24\n"
    "v_add_f32 v25, 1.0, v25\n"
    "v_rcp_f32 v24, v24\n"
    "v_rcp_f32 v25, v25\n"
    "v_mul_f32 v24, v78, v24\n"
    "v_mul_f32 v25, v79, v25\n"
    "v_cvt_pk_bf16_f32 v39, v24, v25\n"
    "s_add_u32 s71, s79, 28672\n"
    "buffer_store_dwordx4 v[32:35], v0, s[16:19], s71 offen offset:0 sc1\n"
    "buffer_store_dwordx4 v[36:39], v0, s[16:19], s71 offen offset:1024 sc1\n"
    "s_cmp_eq_u32 s58, 3\n"
    "s_cbranch_scc0 .Lw7_%=\n"
    "s_waitcnt vmcnt(4)\n"
    "v_cmp_eq_u64 vcc, s[52:53], v[26:27]\n"
    "s_nop 0\n"
    "s_andn2_b32 s70, 127, vcc_lo\n"
    "s_cbranch_scc1 .Lspin12_%=\n"
    ".Lok12_%=:\n"
    ".Lw7_%=:\n"
    "s_waitcnt vmcnt(10)\n"
    "s_barrier\n"
    ".Lq_%=:\n"
    "s_setprio 1\n"
    "ds_read_b64_tr_b16 v[80:81], v13 offset:0\n"
    "ds_read_b64_tr_b16 v[82:83], v13 offset:256\n"
    "ds_read_b64_tr_b16 v[84:85], v13 offset:1024\n"
    "ds_read_b64_tr_b16 v[86:87], v13 offset:1280\n"
    "ds_read_b64_tr_b16 v[88:89], v13 offset:2048\n"
    "ds_read_b64_tr_b16 v[90:91], v13 offset:2304\n"
    "ds_read_b64_tr_b16 v[92:93], v13 offset:3072\n"
    "ds_read_b64_tr_b16 v[94:95], v13 offset:3328\n"
    "ds_read_b64_tr_b16 v[96:97], v13 offset:4096\n"
    "ds_read_b64_tr_b16 v[98:99], v13 offset:4352\n"
    "s_waitcnt lgkmcnt(8)\n"
    "v_mfma_f32_32x32x16_bf16 v[48:63], v[80:83], v[144:147], 0\n"
    "s_nop 3\n"
    "ds_read_b64_tr_b16 v[100:101], v13 offset:5120\n"
    "ds_read_b64_tr_b16 v[102:103], v13 offset:5376\n"
    "s_waitcnt lgkmcnt(8)\n"
    "v_mfma_f32_32x32x16_bf16 v[48:63], v[84:87], v[148:151], v[48:63]\n"
    "buffer_load_dwordx4 v[112:115], v4, s[16:19], s37 offen offset:0 sc1\n"
    "ds_read_b64_tr_b16 v[80:81], v13 offset:6144\n"
    "ds_read_b64_tr_b16 v[82:83], v13 offset:6400\n"
    "s_waitcnt lgkmcnt(8)\n"
    "v_mfma_f32_32x32x16_bf16 v[48:63], v[88:91], v[152:155], v[48:63]\n"
    "s_nop 3\n"
    "ds_read_b64_tr_b16 v[84:85], v13 offset:7168\n"
    "ds_read_b64_tr_b16 v[86:87], v13 offset:7424\n"
    "s_waitcnt lgkmcnt(8)\n"
    "v_mfma_f32_32x32x16_bf16 v[48:63], v[92:95], v[156:159], v[48:63]\n"
    "s_nop 3\n"
    "ds_read_b64_tr_b16 v[88:89], v13 offset:8192\n"
    "ds_read_b64_tr_b16 v[90:91], v13 offset:8448\n"
    "s_waitcnt lgkmcnt(8)\n"
    "v_mfma_f32_32x32x16_bf16 v[48:63], v[96:99], v[160:163], v[48:63]\n"
    "buffer_load_dwordx4 v[116:119], v4, s[16:19], s36 offen offset:0 sc1\n"
    "ds_read_b64_tr_b16 v[92:93], v13 offset:9216\n"
    "ds_read_b64_tr_b16 v[94:95], v13 offset:9472\n"
    "s_waitcnt lgkmcnt(8)\n"
    "v_mfma_f32_32x32x16_bf16 v[48:63], v[100:103], v[164:167], v[48:63]\n"
    "s_nop 3\n"
    "ds_read_b64_tr_b16 v[96:97], v13 offset:10240\n"
    "ds_read_b64_tr_b16 v[98:99], v13 offset:10496\n"
    "s_waitcnt lgkmcnt(8)\n"
    "v_mfma_f32_32x32x16_bf16 v[48:63], v[80:83], v[168:171], v[48:63]\n"
    "s_nop 3\n"
    "ds_read_b64_tr_b16 v[100:101], v13 offset:11264\n"
    "ds_read_b64_tr_b16 v[102:103], v13 offset:11520\n"
    "s_waitcnt lgkmcnt(8)\n"
    "v_mfma_f32_32x32x16_bf16 v[48:63], v[84:87], v[172:175], v[48:63]\n"
    "s_nop 3\n"
    "ds_read_b64_tr_b16 v[80:81], v13 offset:12288\n"
    "ds_read_b64_tr_b16 v[82:83], v13 offset:12544\n"
    "s_waitcnt lgkmcnt(8)\n"
    "v_mfma_f32_32x32x16_bf16 v[48:63], v[88:91], v[176:179], v[48:63]\n"
    "s_nop 3\n"
    "ds_read_b64_tr_b16 v[84:85], v13 offset:13312\n"
    "ds_read_b64_tr_b16 v[86:87], v13 offset:13568\n"
    "s_waitcnt lgkmcnt(8)\n"
    "v_mfma_f32_32x32x16_bf16 v[48:63], v[92:95], v[180:183], v[48:63]\n"
    "s_nop 3\n"
    "ds_read_b64_tr_b16 v[88:89], v13 offset:14336\n"
    "ds_read_b64_tr_b16 v[90:91], v13 offset:14592\n"
    "s_waitcnt lgkmcnt(8)\n"
    "v_mfma_f32_32x32x16_bf16 v[48:63], v[96:99], v[184:187], v[48:63]\n"
    "s_nop 3\n"
    "ds_read_b64_tr_b16 v[92:93], v13 offset:15360\n"
    "ds_read_b64_tr_b16 v[94:95], v13 offset:15616\n"
    "s_waitcnt lgkmcnt(8)\n"
    "v_mfma_f32_32x32x16_bf16 v[48:63], v[100:103], v[188:191], v[48:63]\n"
    "s_nop 3\n"
    "ds_read_b64_tr_b16 v[96:97], v13 offset:16384\n"
    "ds_read_b64_tr_b16 v[98:99], v13 offset:16640\n"
    "s_waitcnt lgkmcnt(8)\n"
    "v_mfma_f32_32x32x16_bf16 v[48:63], v[80:83], v[192:195], v[48:63]\n"
    "s_nop 3\n"
    "ds_read_b64_tr_b16 v[100:101], v13 offset:17408\n"
    "ds_read_b64_tr_b16 v[102:103], v13 offset:17664\n"
    "s_waitcnt lgkmcnt(8)\n"
    "v_mfma_f32_32x32x16_bf16 v[48:63], v[84:87], v[196:199], v[48:63]\n"
    "s_add_u32 s69, s37, 4096\n"
    "buffer_load_dwordx4 v[120:123], v4, s[16:19], s69 offen offset:0 sc1\n"
    "ds_read_b64_tr_b16 v[80:81], v13 offset:18432\n"
    "ds_read_b64_tr_b16 v[82:83], v13 offset:18688\n"
    "s_waitcnt lgkmcnt(8)\n"
    "v_mfma_f32_32x32x16_bf16 v[48:63], v[88:91], v[200:203], v[48:63]\n"
    "s_nop 3\n"
    "ds_read_b64_tr_b16 v[84:85], v13 offset:19456\n"
    "ds_read_b64_tr_b16 v[86:87], v13 offset:19712\n"
    "s_waitcnt lgkmcnt(8)\n"
    "v_mfma_f32_32x32x16_bf16 v[48:63], v[92:95], v[204:207], v[48:63]\n"
    "s_nop 3\n"
    "ds_read_b64_tr_b16 v[88:89], v13 offset:20480\n"
    "ds_read_b64_tr_b16 v[90:91], v13 offset:20736\n"
    "s_waitcnt lgkmcnt(8)\n"
    "v_mfma_f32_32x32x16_bf16 v[48:63], v[96:99], v[208:211], v[48:63]\n"
    "s_add_u32 s69, s36, 4096\n"
    "buffer_load_dwordx4 v[124:127], v4, s[16:19], s69 offen offset:0 sc1\n"
    "ds_read_b64_tr_b16 v[92:93], v13 offset:21504\n"
    "ds_read_b64_tr_b16 v[94:95], v13 offset:21760\n"
    "s_waitcnt lgkmcnt(8)\n"
    "v_mfma_f32_32x32x16_bf16 v[48:63], v[100:103], v[212:215], v[48:63]\n"
    "s_nop 3\n"
    "ds_read_b64_tr_b16 v[96:97], v13 offset:22528\n"
    "ds_read_b64_tr_b16 v[98:99], v13 offset:22784\n"
    "s_waitcnt lgkmcnt(8)\n"
    "v_mfma_f32_32x32x16_bf16 v[48:63], v[80:83], v[216:219], v[48:63]\n"
    "s_add_u32 s69, s36, 8192\n"
    "buffer_load_dwordx4 v[132:135], v4, s[16:19], s69 offen offset:0 sc1\n"
    "ds_read_b64_tr_b16 v[100:101], v13 offset:23552\n"
    "ds_read_b64_tr_b16 v[102:103], v13 offset:23808\n"
    "s_waitcnt lgkmcnt(8)\n"
    "v_mfma_f32_32x32x16_bf16 v[48:63], v[84:87], v[220:223], v[48:63]\n"
    "s_add_u32 m0, s85, 24576\n"
    "s_add_u32 s70, s75, 57344\n"
    "buffer_load_dwordx4 v5, s[20:23], s70 offen lds\n"
    "ds_read_b64_tr_b16 v[80:81], v13 offset:24576\n"
    "ds_read_b64_tr_b16 v[82:83], v13 offset:24832\n"
    "s_waitcnt lgkmcnt(8)\n"
    "v_mfma_f32_32x32x16_bf16 v[48:63], v[88:91], v[224:227], v[48:63]\n"
    "s_nop 3\n"
    "ds_read_b64_tr_b16 v[84:85], v13 offset:25600\n"
    "ds_read_b64_tr_b16 v[86:87], v13 offset:25856\n"
    "s_waitcnt lgkmcnt(8)\n"
    "v_mfma_f32_32x32x16_bf16 v[48:63], v[92:95], v[228:231], v[48:63]\n"
    "s_add_u32 s69, s37, 8192\n"
    "buffer_load_dwordx4 v[128:131], v4, s[16:19], s69 offen offset:0 sc1\n"
    "ds_read_b64_tr_b16 v[88:89], v13 offset:26624\n"
    "ds_read_b64_tr_b16 v[90:91], v13 offset:26880\n"
    "s_waitcnt lgkmcnt(8)\n"
    "v_mfma_f32_32x32x16_bf16 v[48:63], v[96:99], v[232:235], v[48:63]\n"
    "s_add_u32 m0, s85, 25600\n"
    "s_add_u32 s70, s75, 64512\n"
    "buffer_load_dwordx4 v5, s[20:23], s70 offen lds\n"
    "ds_read_b64_tr_b16 v[92:93], v13 offset:27648\n"
    "ds_read_b64_tr_b16 v[94:95], v13 offset:27904\n"
    "s_waitcnt lgkmcnt(8)\n"
    "v_mfma_f32_32x32x16_bf16 v[48:63], v[100:103], v[236:239], v[48:63]\n"
    "s_nop 3\n"
    "s_waitcnt lgkmcnt(6)\n"
    "v_mfma_f32_32x32x16_bf16 v[48:63], v[80:83], v[240:243], v[48:63]\n"
    "s_nop 5\n"
    "s_waitcnt lgkmcnt(4)\n"
    "v_mfma_f32_32x32x16_bf16 v[48:63], v[84:87], v[244:247], v[48:63]\n"
    "s_nop 5\n"
    "s_waitcnt lgkmcnt(2)\n"
    "v_mfma_f32_32x32x16_bf16 v[48:63], v[88:91], v[248:251], v[48:63]\n"
    "s_nop 5\n"
    "s_waitcnt lgkmcnt(0)\n"
    "v_mfma_f32_32x32x16_bf16 v[48:63], v[92:95], v[252:255], v[48:63]\n"
    "ds_read_b64_tr_b16 v[40:41], v15 offset:0\n"
    "ds_read_b64_tr_b16 v[42:43], v15 offset:448\n"
    "ds_read_b64_tr_b16 v[44:45], v15 offset:896\n"
    "ds_read_b64_tr_b16 v[46:47], v15 offset:1344\n"
    "s_nop 7\n"
    "v_cvt_pk_bf16_f32 v32, v48, v49\n"
    "v_cvt_pk_bf16_f32 v33, v50, v51\n"
    "v_cvt_pk_bf16_f32 v34, v52, v53\n"
    "v_cvt_pk_bf16_f32 v35, v54, v55\n"
    "v_cvt_pk_bf16_f32 v36, v56, v57\n"
    "v_cvt_pk_bf16_f32 v37, v58, v59\n"
    "v_cvt_pk_bf16_f32 v38, v60, v61\n"
    "v_cvt_pk_bf16_f32 v39, v62, v63\n"
    "s_waitcnt lgkmcnt(0)\n"
    "v_cndmask_b32 v46, v46, 0, s[54:55]\n"
    "v_cndmask_b32 v47, v47, 0, s[54:55]\n"
    "v_mfma_f32_32x32x16_bf16 v[208:223], v[40:43], v[32:35], 0\n"
    "s_nop 0\n"
    "v_mfma_f32_32x32x16_bf16 v[208:223], v[44:47], v[36:39], v[208:223]\n"
    "v_max_f32 v14, 0xc2700000, v60\n"
    "v_min_f32 v14, 0x42700000, v14\n"
    "v_mul_f32 v14, s87, v14\n"
    "v_exp_f32 v14, v14\n"
    "s_cmp_eq_u32 s61, 0\n"
    "s_cselect_b64 vcc, s[54:55], -1\n"
    "v_mov_b32 v24, v14\n"
    "v_lshlrev_b32 v25, 2, v30\n"
    "v_cndmask_b32 v25, v25, v28, vcc\n"
    "v_permlane32_swap_b32 v24, v14\n"
    "v_add_f32 v24, 1.0, v14\n"
    "v_rcp_f32 v24, v24\n"
    "s_nop 0\n"
    "v_and_b32 v24, 0xffffff00, v24\n"
    "v_or_b32 v24, s12, v24\n"
    "buffer_store_dword v24, v25, s[16:19], s82 offen sc1\n"
    "v_max3_f32 v22, v208, v209, v210\n"
    "v_max3_f32 v23, -v208, -v209, -v210\n"
    "v_max3_f32 v22, v22, v211, v212\n"
    "v_max3_f32 v23, v23, -v211, -v212\n"
    "v_max3_f32 v22, v22, v213, v214\n"
    "v_max3_f32 v23, v23, -v213, -v214\n"
    "v_max3_f32 v22, v22, v215, v216\n"
    "v_max3_f32 v23, v23, -v215, -v216\n"
    "v_max3_f32 v22, v22, v217, v218\n"
    "v_max3_f32 v23, v23, -v217, -v218\n"
    "v_max3_f32 v22, v22, v219, v220\n"
    "v_max3_f32 v23, v23, -v219, -v220\n"
    "v_max3_f32 v22, v22, v221, v222\n"
    "v_max3_f32 v23, v23, -v221, -v222\n"
    "v_max_f32 v22, v22, v223\n"
    "v_max_f32_e64 v23, v23, -v223\n"
    "s_nop 1\n"
    "v_permlane32_swap_b32 v22, v23\n"
    "v_max_f32 v22, v22, v23\n"
    "v_mul_f32 v22, 0xbfb8aa3b, v22\n"
    "v_mov_b32 v23, v22\n"
    "s_nop 1\n"
    "v_permlane32_swap_b32 v22, v23\n"
    "v_fma_f32 v224, v208, s87, v22\n"
    "v_fma_f32 v240, -v208, s87, v23\n"
    "v_fma_f32 v225, v209, s87, v22\n"
    "v_fma_f32 v241, -v209, s87, v23\n"
    "v_exp_f32 v224, v224\n"
    "v_exp_f32 v240, v240\n"
    "v_exp_f32 v225, v225\n"
    "v_exp_f32 v241, v241\n"
    "v_fma_f32 v226, v210, s87, v22\n"
    "v_fma_f32 v242, -v210, s87, v23\n"
    "v_fma_f32 v227, v211, s87, v22\n"
    "v_fma_f32 v243, -v211, s87, v23\n"
    "v_exp_f32 v226, v226\n"
    "v_exp_f32 v242, v242\n"
    "v_exp_f32 v227, v227\n"
    "v_exp_f32 v243, v243\n"
    "v_fma_f32 v228, v212, s87, v22\n"
    "v_fma_f32 v244, -v212, s87, v23\n"
    "v_fma_f32 v229, v213, s87, v22\n"
    "v_fma_f32 v245, -v213, s87, v23\n"
    "v_exp_f32 v228, v228\n"
    "v_exp_f32 v244, v244\n"
    "v_exp_f32 v229, v229\n"
    "v_exp_f32 v245, v245\n"
    "v_fma_f32 v230, v214, s87, v22\n"
    "v_fma_f32 v246, -v214, s87, v23\n"
    "v_fma_f32 v231, v215, s87, v22\n"
    "v_fma_f32 v247, -v215, s87, v23\n"
    "v_exp_f32 v230, v230\n"
    "v_exp_f32 v246, v246\n"
    "v_exp_f32 v231, v231\n"
    "v_exp_f32 v247, v247\n"
    "v_fma_f32 v232, v216, s87, v22\n"
    "v_fma_f32 v248, -v216, s87, v23\n"
    "v_fma_f32 v233, v217, s87, v22\n"
    "v_fma_f32 v249, -v217, s87, v23\n"
    "v_exp_f32 v232, v232\n"
    "v_exp_f32 v248, v248\n"
    "v_exp_f32 v233, v233\n"
    "v_exp_f32 v249, v249\n"
    "v_fma_f32 v234, v218, s87, v22\n"
    "v_fma_f32 v250, -v218, s87, v23\n"
    "v_fma_f32 v235, v219, s87, v22\n"
    "v_fma_f32 v251, -v219, s87, v23\n"
    "v_exp_f32 v234, v234\n"
    "v_exp_f32 v250, v250\n"
    "v_exp_f32 v235, v235\n"
    "v_exp_f32 v251, v251\n"
    "v_fma_f32 v236, v220, s87, v22\n"
    "v_fma_f32 v252, -v220, s87, v23\n"
    "v_fma_f32 v237, v221, s87, v22\n"
    "v_fma_f32 v253, -v221, s87, v23\n"
    "v_exp_f32 v236, v236\n"
    "v_exp_f32 v252, v252\n"
    "v_exp_f32 v237, v237\n"
    "v_exp_f32 v253, v253\n"
    "v_fma_f32 v238, v222, s87, v22\n"
    "v_fma_f32 v254, -v222, s87, v23\n"
    "v_fma_f32 v239, v223, s87, v22\n"
    "v_fma_f32 v255, -v223, s87, v23\n"
    "v_exp_f32 v238, v238\n"
    "v_exp_f32 v254, v254\n"
    "v_exp_f32 v239, v239\n"
    "v_exp_f32 v255, v255\n"
    "v_cvt_pk_bf16_f32 v64, v224, v225\n"
    "v_cvt_pk_bf16_f32 v65, v226, v227\n"
    "v_cvt_pk_bf16_f32 v66, v228, v229\n"
    "v_cvt_pk_bf16_f32 v67, v230, v231\n"
    "v_cvt_pk_bf16_f32 v68, v232, v233\n"
    "v_cvt_pk_bf16_f32 v69, v234, v235\n"
    "v_cvt_pk_bf16_f32 v70, v236, v237\n"
    "v_cvt_pk_bf16_f32 v71, v238, v239\n"
    "v_cvt_pk_bf16_f32 v72, v240, v241\n"
    "v_cvt_pk_bf16_f32 v73, v242, v243\n"
    "v_cvt_pk_bf16_f32 v74, v244, v245\n"
    "v_cvt_pk_bf16_f32 v75, v246, v247\n"
    "v_cvt_pk_bf16_f32 v76, v248, v249\n"
    "v_cvt_pk_bf16_f32 v77, v250, v251\n"
    "v_cvt_pk_bf16_f32 v78, v252, v253\n"
    "v_cvt_pk_bf16_f32 v79, v254, v255\n"
    "s_setprio 0\n"
    "s_cmp_eq_u32 s64, 1\n"
    "s_cbranch_scc1 .Lsp1o_%=\n"
    "v_xor_b32 v2, 64, v141\n"
    "v_add_u32 v3, 57344, v141\n"
    "v_add_u32 v4, 57344, v2\n"
    "v_lshrrev_b32 v23, 5, v30\n"
    "v_and_b32 v22, 0x70, v0\n"
    "v_add_u32 v22, 163072, v22\n"
    "s_mul_i32 s70, s58, 12288\n"
    "s_add_u32 s70, s70, 106496\n"
    "v_bfe_u32 v24, v30, 2, 1\n"
    "v_xor_b32 v24, 1, v24\n"
    "v_and_b32 v24, v24, v23\n"
    "v_lshlrev_b32 v24, 6, v24\n"
    "v_xor_b32 v13, v24, v141\n"
    "v_mul_u32_u24 v23, 384, v23\n"
    "v_add3_u32 v13, v13, v23, s70\n"
    "v_xor_b32 v15, 64, v13\n"
    "v_min_u32 v21, 150776, v13\n"
    "v_min_u32 v6, 150776, v15\n"
    "ds_read_b128 v[8:11], v22\n"
    "s_waitcnt vmcnt(9)\n"
    "s_barrier\n"
    "ds_read_b64_tr_b16 v[80:81], v3 offset:24576\n"
    "ds_read_b64_tr_b16 v[82:83], v3 offset:25600\n"
    "ds_read_b64_tr_b16 v[84:85], v4 offset:24576\n"
    "ds_read_b64_tr_b16 v[86:87], v4 offset:25600\n"
    "ds_read_b64_tr_b16 v[88:89], v3 offset:32768\n"
    "ds_read_b64_tr_b16 v[90:91], v3 offset:33792\n"
    "ds_read_b64_tr_b16 v[92:93], v4 offset:32768\n"
    "ds_read_b64_tr_b16 v[94:95], v4 offset:33792\n"
    "s_add_u32 m0, s85, 40960\n"
    "s_add_u32 s70, s77, 57344\n"
    "buffer_load_dwordx4 v5, s[20:23], s70 offen lds\n"
    "buffer_load_dwordx2 v[26:27], v143, s[16:19], s38 offen offset:2048 sc1\n"
    "ds_read_b64_tr_b16 v[96:97], v3 offset:40960\n"
    "ds_read_b64_tr_b16 v[98:99], v3 offset:41984\n"
    "ds_read_b64_tr_b16 v[100:101], v4 offset:40960\n"
    "ds_read_b64_tr_b16 v[102:103], v4 offset:41984\n"
    "s_waitcnt vmcnt(10) lgkmcnt(8)\n"
    "v_mfma_f32_32x32x16_bf16 v[176:191], v[80:83], v[112:115], 0\n"
    "v_mfma_f32_32x32x16_bf16 v[192:207], v[84:87], v[112:115], 0\n"
    "s_add_u32 m0, s85, 49152\n"
    "s_add_u32 s70, s78, 57344\n"
    "buffer_load_dwordx4 v5, s[20:23], s70 offen lds\n"
    "v_lshrrev_b32 v111, 3, v30\n"
    "v_and_b32 v110, 7, v30\n"
    "v_mul_u32_u24 v109, 896, v111\n"
    "v_lshl_add_u32 v109, v110, 4, v109\n"
    "v_lshl_add_u32 v111, s58, 3, v111\n"
    "v_mul_u32_u24 v108, 1088, v110\n"
    "v_lshlrev_b32 v110, 10, v110\n"
    "v_lshl_add_u32 v108, v111, 4, v108\n"
    "v_lshl_add_u32 v110, v111, 4, v110\n"
    "v_lshlrev_b32 v111, 2, v111\n"
    "v_add_u32 v108, 34816, v108\n"
    "s_lshl2_add_u32 s71, s56, s58\n"
    "s_xor_b32 s70, s71, 0\n"
    "s_mul_i32 s70, s70, 2176\n"
    "v_add_u32 v104, s70, v0\n"
    "s_xor_b32 s70, s71, 1\n"
    "s_mul_i32 s70, s70, 2176\n"
    "v_add_u32 v105, s70, v0\n"
    "s_xor_b32 s70, s71, 2\n"
    "s_mul_i32 s70, s70, 2176\n"
    "v_add_u32 v106, s70, v0\n"
    "s_xor_b32 s70, s71, 3\n"
    "s_mul_i32 s70, s70, 2176\n"
    "v_add_u32 v107, s70, v0\n"
    "s_waitcnt vmcnt(1)\n"
    "v_cmp_eq_u64 vcc, s[52:53], v[26:27]\n"
    "s_nop 0\n"
    "s_andn2_b32 s70, 127, vcc_lo\n"
    "s_cbranch_scc1 .Lspin13_%=\n"
    ".Lok13_%=:\n"
    "s_add_u32 s38, s38, 4096\n"
    "s_add_u32 s69, s37, 12288\n"
    "buffer_load_dwordx4 v[136:139], v0, s[16:19], s69 offen offset:0 sc1\n"
    "s_add_u32 s69, s36, 12288\n"
    "buffer_load_dwordx4 v[144:147], v0, s[16:19], s69 offen offset:0 sc1\n"
    "s_add_u32 s69, s37, 16384\n"
    "buffer_load_dwordx4 v[148:151], v0, s[16:19], s69 offen offset:0 sc1\n"
    "s_add_u32 s69, s36, 16384\n"
    "buffer_load_dwordx4 v[152:155], v0, s[16:19], s69 offen offset:0 sc1\n"
    "s_add_u32 s69, s37, 20480\n"
    "buffer_load_dwordx4 v[156:159], v0, s[16:19], s69 offen offset:0 sc1\n"
    "s_waitcnt vmcnt(15)\n"
    "v_mfma_f32_32x32x16_bf16 v[32:47], v[80:83], v[116:119], 0\n"
    "v_mfma_f32_32x32x16_bf16 v[48:63], v[84:87], v[116:119], 0\n"
    "s_add_u32 m0, s93, 135168\n"
    "s_add_u32 s70, s45, 200704\n"
    "buffer_load_dwordx4 v5, s[24:27], s70 offen lds\n"
    "ds_read_b64_tr_b16 v[80:81], v3 offset:49152\n"
    "ds_read_b64_tr_b16 v[82:83], v3 offset:50176\n"
    "ds_read_b64_tr_b16 v[84:85], v4 offset:49152\n"
    "ds_read_b64_tr_b16 v[86:87], v4 offset:50176\n"
    "s_waitcnt vmcnt(15) lgkmcnt(8)\n"
    "v_mfma_f32_32x32x16_bf16 v[176:191], v[88:91], v[120:123], v[176:191]\n"
    "v_mfma_f32_32x32x16_bf16 v[192:207], v[92:95], v[120:123], v[192:207]\n"
    "s_add_u32 m0, s93, 139264\n"
    "s_add_u32 s70, s45, 229376\n"
    "buffer_load_dwordx4 v5, s[24:27], s70 offen lds\n"
    "s_add_u32 m0, s93, 131072\n"
    "s_add_u32 s70, s45, 172032\n"
    "buffer_load_dwordx4 v5, s[24:27], s70 offen lds\n"
    "s_waitcnt vmcnt(16)\n"
    "v_mfma_f32_32x32x16_bf16 v[32:47], v[88:91], v[124:127], v[32:47]\n"
    "v_mfma_f32_32x32x16_bf16 v[48:63], v[92:95], v[124:127], v[48:63]\n"
    "ds_read_b64_tr_b16 v[88:89], v3 offset:0\n"
    "ds_read_b64_tr_b16 v[90:91], v3 offset:1024\n"
    "ds_read_b64_tr_b16 v[92:93], v4 offset:0\n"
    "ds_read_b64_tr_b16 v[94:95], v4 offset:1024\n"
    "s_waitcnt vmcnt(13) lgkmcnt(8)\n"
    "v_mfma_f32_32x32x16_bf16 v[176:191], v[96:99], v[128:131], v[176:191]\n"
    "v_mfma_f32_32x32x16_bf16 v[192:207], v[100:103], v[128:131], v[192:207]\n"
    "s_add_u32 m0, s93, 143360\n"
    "s_add_u32 s70, s45, 258048\n"
    "buffer_load_dwordx4 v5, s[24:27], s70 offen lds\n"
    "s_waitcnt vmcnt(16)\n"
    "v_mfma_f32_32x32x16_bf16 v[32:47], v[96:99], v[132:135], v[32:47]\n"
    "v_mfma_f32_32x32x16_bf16 v[48:63], v[100:103], v[132:135], v[48:63]\n"
    "s_add_u32 s69, s37, 24576\n"
    "buffer_load_dwordx4 v[164:167], v0, s[16:19], s69 offen offset:0 sc1\n"
    "ds_read_b64_tr_b16 v[96:97], v3 offset:8192\n"
    "ds_read_b64_tr_b16 v[98:99], v3 offset:9216\n"
    "ds_read_b64_tr_b16 v[100:101], v4 offset:8192\n"
    "ds_read_b64_tr_b16 v[102:103], v4 offset:9216\n"
    "s_waitcnt vmcnt(9) lgkmcnt(8)\n"
    "v_mfma_f32_32x32x16_bf16 v[176:191], v[80:83], v[136:139], v[176:191]\n"
    "v_mfma_f32_32x32x16_bf16 v[192:207], v[84:87], v[136:139], v[192:207]\n"
    "s_setprio 0\n"
    "s_waitcnt vmcnt(8)\n"
    "v_mfma_f32_32x32x16_bf16 v[32:47], v[80:83], v[144:147], v[32:47]\n"
    "v_mfma_f32_32x32x16_bf16 v[48:63], v[84:87], v[144:147], v[48:63]\n"
    "s_add_u32 s69, s36, 24576\n"
    "buffer_load_dwordx4 v[168:171], v0, s[16:19], s69 offen offset:0 sc1\n"
    "ds_read_b64_tr_b16 v[80:81], v3 offset:16384\n"
    "ds_read_b64_tr_b16 v[82:83], v3 offset:17408\n"
    "ds_read_b64_tr_b16 v[84:85], v4 offset:16384\n"
    "ds_read_b64_tr_b16 v[86:87], v4 offset:17408\n"
    "s_waitcnt vmcnt(8) lgkmcnt(8)\n"
    "v_mfma_f32_32x32x16_bf16 v[176:191], v[88:91], v[148:151], v[176:191]\n"
    "v_mfma_f32_32x32x16_bf16 v[192:207], v[92:95], v[148:151], v[192:207]\n"
    "s_add_u32 s69, s36, 20480\n"
    "buffer_load_dwordx4 v[160:163], v0, s[16:19], s69 offen offset:0 sc1\n"
    "s_waitcnt vmcnt(8)\n"
    "v_mfma_f32_32x32x16_bf16 v[32:47], v[88:91], v[152:155], v[32:47]\n"
    "v_mfma_f32_32x32x16_bf16 v[48:63], v[92:95], v[152:155], v[48:63]\n"
    "s_waitcnt vmcnt(12) lgkmcnt(0)\n"
    "s_barrier\n"
    "v_add_u32 v1, 155648, v0\n"
    "ds_read_b128 v[208:211], v1 offset:0\n"
    "ds_read_b128 v[212:215], v1 offset:1024\n"
    "ds_read_b128 v[216:219], v1 offset:2048\n"
    "ds_read_b128 v[220:223], v1 offset:3072\n"
    "ds_read_b64_tr_b16 v[88:89], v141 offset:0\n"
    "ds_read_b64_tr_b16 v[90:91], v141 offset:1024\n"
    "ds_read_b64_tr_b16 v[92:93], v2 offset:0\n"
    "ds_read_b64_tr_b16 v[94:95], v2 offset:1024\n"
    "s_waitcnt vmcnt(7) lgkmcnt(12)\n"
    "v_mfma_f32_32x32x16_bf16 v[176:191], v[96:99], v[156:159], v[176:191]\n"
    "v_mfma_f32_32x32x16_bf16 v[192:207], v[100:103], v[156:159], v[192:207]\n"
    "s_waitcnt lgkmcnt(4)\n"
    "v_mfma_f32_32x32x16_bf16 v[224:239], v[208:211], v[64:67], 0\n"
    "v_mfma_f32_32x32x16_bf16 v[240:255], v[216:219], v[72:75], 0\n"
    "v_mfma_f32_32x32x16_bf16 v[224:239], v[212:215], v[68:71], v[224:239]\n"
    "v_mfma_f32_32x32x16_bf16 v[240:255], v[220:223], v[76:79], v[240:255]\n"
    "s_nop 10\n"
    "v_rcp_f32 v22, v239\n"
    "v_rcp_f32 v23, v255\n"
    "v_fmaak_f32 v24, v231, v22, 0x358637bd\n"
    "v_fmac_f32 v24, v247, v23\n"
    "v_rcp_f32 v24, v24\n"
    "s_nop 0\n"
    "v_mul_f32 v24, v14, v24\n"
    "v_mul_f32 v22, v24, v22\n"
    "v_mul_f32 v23, v24, v23\n"
    "v_mul_f32 v224, v22, v224\n"
    "v_mul_f32 v225, v22, v225\n"
    "v_fmac_f32 v224, v23, v240\n"
    "v_fmac_f32 v225, v23, v241\n"
    "v_cvt_pk_bf16_f32 v208, v224, v225\n"
    "v_mul_f32 v226, v22, v226\n"
    "v_mul_f32 v227, v22, v227\n"
    "v_fmac_f32 v226, v23, v242\n"
    "v_fmac_f32 v227, v23, v243\n"
    "v_cvt_pk_bf16_f32 v209, v226, v227\n"
    "v_mul_f32 v228, v22, v228\n"
    "v_mul_f32 v229, v22, v229\n"
    "v_fmac_f32 v228, v23, v244\n"
    "v_fmac_f32 v229, v23, v245\n"
    "v_cvt_pk_bf16_f32 v210, v228, v229\n"
    "v_mul_f32 v230, v22, v230\n"
    "v_fmac_f32 v230, v23, v246\n"
    "v_cvt_pk_bf16_f32 v211, v230, 0\n"
    "v_or_b32 v211, s47, v211\n"
    "v_mul_f32 v232, v22, v232\n"
    "v_mul_f32 v233, v22, v233\n"
    "v_fmac_f32 v232, v23, v248\n"
    "v_fmac_f32 v233, v23, v249\n"
    "v_cvt_pk_bf16_f32 v212, v232, v233\n"
    "v_mul_f32 v234, v22, v234\n"
    "v_mul_f32 v235, v22, v235\n"
    "v_fmac_f32 v234, v23, v250\n"
    "v_fmac_f32 v235, v23, v251\n"
    "v_cvt_pk_bf16_f32 v213, v234, v235\n"
    "v_mul_f32 v236, v22, v236\n"
    "v_mul_f32 v237, v22, v237\n"
    "v_fmac_f32 v236, v23, v252\n"
    "v_fmac_f32 v237, v23, v253\n"
    "v_cvt_pk_bf16_f32 v214, v236, v237\n"
    "v_mul_f32 v238, v22, v238\n"
    "v_fmac_f32 v238, v23, v254\n"
    "v_cvt_pk_bf16_f32 v215, v238, 0\n"
    "v_or_b32 v215, s47, v215\n"
    "buffer_store_dwordx4 v[208:211], v0, s[16:19], s81 offen offset:0 sc1\n"
    "buffer_store_dwordx4 v[212:215], v0, s[16:19], s81 offen offset:1024 sc1\n"
    "s_add_u32 s69, s37, 28672\n"
    "buffer_load_dwordx4 v[172:175], v0, s[16:19], s69 offen offset:0 sc1\n"
    "s_waitcnt vmcnt(3)\n"
    "v_mfma_f32_32x32x16_bf16 v[32:47], v[96:99], v[160:163], v[32:47]\n"
    "v_mfma_f32_32x32x16_bf16 v[48:63], v[100:103], v[160:163], v[48:63]\n"
    "s_add_u32 s69, s37, 32768\n"
    "buffer_load_dwordx4 v[112:115], v0, s[16:19], s69 offen offset:0 sc1\n"
    "s_add_u32 s69, s37, 36864\n"
    "buffer_load_dwordx4 v[116:119], v0, s[16:19], s69 offen offset:0 sc1\n"
    "s_add_u32 s69, s37, 40960\n"
    "buffer_load_dwordx4 v[120:123], v0, s[16:19], s69 offen offset:0 sc1\n"
    "s_add_u32 s69, s37, 45056\n"
    "buffer_load_dwordx4 v[124:127], v0, s[16:19], s69 offen offset:0 sc1\n"
    "s_add_u32 s69, s37, 49152\n"
    "buffer_load_dwordx4 v[128:131], v0, s[16:19], s69 offen offset:0 sc1\n"
    "s_add_u32 s69, s37, 53248\n"
    "buffer_load_dwordx4 v[132:135], v0, s[16:19], s69 offen offset:0 sc1\n"
    "ds_read_b64_tr_b16 v[96:97], v141 offset:8192\n"
    "ds_read_b64_tr_b16 v[98:99], v141 offset:9216\n"
    "ds_read_b64_tr_b16 v[100:101], v2 offset:8192\n"
    "ds_read_b64_tr_b16 v[102:103], v2 offset:9216\n"
    "s_waitcnt vmcnt(11) lgkmcnt(12)\n"
    "v_mfma_f32_32x32x16_bf16 v[176:191], v[80:83], v[164:167], v[176:191]\n"
    "v_mfma_f32_32x32x16_bf16 v[192:207], v[84:87], v[164:167], v[192:207]\n"
    "s_setprio 1\n"
    "s_waitcnt vmcnt(10)\n"
    "v_mfma_f32_32x32x16_bf16 v[32:47], v[80:83], v[168:171], v[32:47]\n"
    "v_mfma_f32_32x32x16_bf16 v[48:63], v[84:87], v[168:171], v[48:63]\n"
    "s_add_u32 s69, s36, 53248\n"
    "buffer_load_dwordx4 v[164:167], v0, s[16:19], s69 offen offset:0 sc1\n"
    "s_add_u32 s69, s36, 49152\n"
    "buffer_load_dwordx4 v[160:163], v0, s[16:19], s69 offen offset:0 sc1\n"
    "s_add_u32 s69, s36, 32768\n"
    "buffer_load_dwordx4 v[144:147], v0, s[16:19], s69 offen offset:0 sc1\n"
    "s_add_u32 s69, s36, 28672\n"
    "buffer_load_dwordx4 v[136:139], v0, s[16:19], s69 offen offset:0 sc1\n"
    "ds_read_b64_tr_b16 v[80:81], v141 offset:16384\n"
    "ds_read_b64_tr_b16 v[82:83], v141 offset:17408\n"
    "ds_read_b64_tr_b16 v[84:85], v2 offset:16384\n"
    "ds_read_b64_tr_b16 v[86:87], v2 offset:17408\n"
    "s_waitcnt vmcnt(10) lgkmcnt(8)\n"
    "v_mfma_f32_32x32x16_bf16 v[176:191], v[88:91], v[172:175], v[176:191]\n"
    "v_mfma_f32_32x32x16_bf16 v[192:207], v[92:95], v[172:175], v[192:207]\n"
    "s_add_u32 s69, s36, 40960\n"
    "buffer_load_dwordx4 v[152:155], v0, s[16:19], s69 offen offset:0 sc1\n"
    "ds_read_b64_tr_b16 v[88:89], v141 offset:24576\n"
    "ds_read_b64_tr_b16 v[90:91], v141 offset:25600\n"
    "ds_read_b64_tr_b16 v[92:93], v2 offset:24576\n"
    "ds_read_b64_tr_b16 v[94:95], v2 offset:25600\n"
    "s_waitcnt vmcnt(10) lgkmcnt(8)\n"
    "v_mfma_f32_32x32x16_bf16 v[176:191], v[96:99], v[112:115], v[176:191]\n"
    "v_mfma_f32_32x32x16_bf16 v[192:207], v[100:103], v[112:115], v[192:207]\n"
    "s_add_u32 s69, s36, 36864\n"
    "buffer_load_dwordx4 v[148:151], v0, s[16:19], s69 offen offset:0 sc1\n"
    "ds_read_b64_tr_b16 v[96:97], v141 offset:32768\n"
    "ds_read_b64_tr_b16 v[98:99], v141 offset:33792\n"
    "ds_read_b64_tr_b16 v[100:101], v2 offset:32768\n"
    "ds_read_b64_tr_b16 v[102:103], v2 offset:33792\n"
    "s_waitcnt vmcnt(10) lgkmcnt(8)\n"
    "v_mfma_f32_32x32x16_bf16 v[176:191], v[80:83], v[116:119], v[176:191]\n"
    "v_mfma_f32_32x32x16_bf16 v[192:207], v[84:87], v[116:119], v[192:207]\n"
    "ds_read_b64_tr_b16 v[80:81], v141 offset:40960\n"
    "ds_read_b64_tr_b16 v[82:83], v141 offset:41984\n"
    "ds_read_b64_tr_b16 v[84:85], v2 offset:40960\n"
    "ds_read_b64_tr_b16 v[86:87], v2 offset:41984\n"
    "s_waitcnt vmcnt(9) lgkmcnt(8)\n"
    "v_mfma_f32_32x32x16_bf16 v[176:191], v[88:91], v[120:123], v[176:191]\n"
    "v_mfma_f32_32x32x16_bf16 v[192:207], v[92:95], v[120:123], v[192:207]\n"
    "s_add_u32 s69, s36, 45056\n"
    "buffer_load_dwordx4 v[156:159], v0, s[16:19], s69 offen offset:0 sc1\n"
    "s_setprio 1\n"
    "ds_read_b64_tr_b16 v[88:89], v141 offset:49152\n"
    "ds_read_b64_tr_b16 v[90:91], v141 offset:50176\n"
    "ds_read_b64_tr_b16 v[92:93], v2 offset:49152\n"
    "ds_read_b64_tr_b16 v[94:95], v2 offset:50176\n"
    "s_waitcnt vmcnt(9) lgkmcnt(8)\n"
    "v_mfma_f32_32x32x16_bf16 v[176:191], v[96:99], v[124:127], v[176:191]\n"
    "v_mfma_f32_32x32x16_bf16 v[192:207], v[100:103], v[124:127], v[192:207]\n"
    "ds_read_b64_tr_b16 v[96:97], v141 offset:0\n"
    "ds_read_b64_tr_b16 v[98:99], v141 offset:1024\n"
    "ds_read_b64_tr_b16 v[100:101], v2 offset:0\n"
    "ds_read_b64_tr_b16 v[102:103], v2 offset:1024\n"
    "s_waitcnt vmcnt(8) lgkmcnt(8)\n"
    "v_mfma_f32_32x32x16_bf16 v[176:191], v[80:83], v[128:131], v[176:191]\n"
    "v_mfma_f32_32x32x16_bf16 v[192:207], v[84:87], v[128:131], v[192:207]\n"
    "s_setprio 0\n"
    "ds_read_b64_tr_b16 v[80:81], v141 offset:8192\n"
    "ds_read_b64_tr_b16 v[82:83], v141 offset:9216\n"
    "ds_read_b64_tr_b16 v[84:85], v2 offset:8192\n"
    "ds_read_b64_tr_b16 v[86:87], v2 offset:9216\n"
    "s_waitcnt vmcnt(7) lgkmcnt(8)\n"
    "v_mfma_f32_32x32x16_bf16 v[176:191], v[88:91], v[132:135], v[176:191]\n"
    "v_mfma_f32_32x32x16_bf16 v[192:207], v[92:95], v[132:135], v[192:207]\n"
    "s_add_u32 s70, s85, 81920\n"
    "v_add_u32 v1, s70, v0\n"
    "v_xor_b32 v22, 0x800, v1\n"
    "v_xor_b32 v23, 0x1000, v1\n"
    "v_xor_b32 v24, 0x1800, v1\n"
    "s_nop 5\n"
    "ds_write_b128 v22, v[184:187] offset:0\n"
    "ds_write_b128 v22, v[188:191] offset:1024\n"
    "ds_write_b128 v23, v[192:195] offset:8192\n"
    "ds_write_b128 v23, v[196:199] offset:9216\n"
    "ds_write_b128 v24, v[200:203] offset:16384\n"
    "ds_write_b128 v24, v[204:207] offset:17408\n"
    "buffer_load_dwordx4 v[168:171], v0, s[16:19], s39 offen offset:0 sc1\n"
    "s_waitcnt lgkmcnt(0)\n"
    "s_barrier\n"
    "buffer_load_dwordx4 v[172:175], v0, s[16:19], s39 offen offset:1024 sc1\n"
    "ds_read_b64_tr_b16 v[88:89], v141 offset:16384\n"
    "ds_read_b64_tr_b16 v[90:91], v141 offset:17408\n"
    "ds_read_b64_tr_b16 v[92:93], v2 offset:16384\n"
    "ds_read_b64_tr_b16 v[94:95], v2 offset:17408\n"
    "s_waitcnt vmcnt(5) lgkmcnt(14)\n"
    "v_mfma_f32_32x32x16_bf16 v[32:47], v[96:99], v[136:139], v[32:47]\n"
    "v_mfma_f32_32x32x16_bf16 v[48:63], v[100:103], v[136:139], v[48:63]\n"
    "ds_read_b128 v[184:187], v1 offset:0\n"
    "ds_read_b128 v[188:191], v1 offset:1024\n"
    "ds_read_b128 v[192:195], v1 offset:8192\n"
    "ds_read_b128 v[196:199], v1 offset:9216\n"
    "ds_read_b128 v[200:203], v1 offset:16384\n"
    "ds_read_b128 v[204:207], v1 offset:17408\n"
    "s_waitcnt lgkmcnt(0)\n"
    "v_mov_b32 v25, 0xff\n"
    "v_add_f32 v176, v176, v184\n"
    "v_add_f32 v176, v176, v192\n"
    "v_add_f32 v176, v176, v200\n"
    "v_bfi_b32 v176, v25, s12, v176\n"
    "v_add_f32 v177, v177, v185\n"
    "v_add_f32 v177, v177, v193\n"
    "v_add_f32 v177, v177, v201\n"
    "v_bfi_b32 v177, v25, s13, v177\n"
    "v_add_f32 v178, v178, v186\n"
    "v_add_f32 v178, v178, v194\n"
    "v_add_f32 v178, v178, v202\n"
    "v_bfi_b32 v178, v25, s14, v178\n"
    "v_add_f32 v179, v179, v187\n"
    "v_add_f32 v179, v179, v195\n"
    "v_add_f32 v179, v179, v203\n"
    "v_bfi_b32 v179, v25, s15, v179\n"
    "buffer_store_dwordx4 v[176:179], v0, s[16:19], s43 offen offset:0 sc1\n"
    "v_add_f32 v180, v180, v188\n"
    "v_add_f32 v180, v180, v196\n"
    "v_add_f32 v180, v180, v204\n"
    "v_bfi_b32 v180, v25, s12, v180\n"
    "v_add_f32 v181, v181, v189\n"
    "v_add_f32 v181, v181, v197\n"
    "v_add_f32 v181, v181, v205\n"
    "v_bfi_b32 v181, v25, s13, v181\n"
    "v_add_f32 v182, v182, v190\n"
    "v_add_f32 v182, v182, v198\n"
    "v_add_f32 v182, v182, v206\n"
    "v_bfi_b32 v182, v25, s14, v182\n"
    "v_add_f32 v183, v183, v191\n"
    "v_add_f32 v183, v183, v199\n"
    "v_add_f32 v183, v183, v207\n"
    "v_bfi_b32 v183, v25, s15, v183\n"
    "buffer_store_dwordx4 v[180:183], v0, s[16:19], s43 offen offset:1024 sc1\n"
    "v_lshlrev_b32 v176, 16, v8\n"
    "v_and_b32 v177, 0xffff0000, v8\n"
    "v_lshlrev_b32 v178, 16, v9\n"
    "v_and_b32 v179, 0xffff0000, v9\n"
    "v_lshlrev_b32 v180, 16, v10\n"
    "v_and_b32 v181, 0xffff0000, v10\n"
    "v_lshlrev_b32 v182, 16, v11\n"
    "v_and_b32 v183, 0xffff0000, v11\n"
    "ds_read_b64_tr_b16 v[96:97], v141 offset:24576\n"
    "ds_read_b64_tr_b16 v[98:99], v141 offset:25600\n"
    "ds_read_b64_tr_b16 v[100:101], v2 offset:24576\n"
    "ds_read_b64_tr_b16 v[102:103], v2 offset:25600\n"
    "s_waitcnt vmcnt(8) lgkmcnt(15)\n"
    "v_mfma_f32_32x32x16_bf16 v[32:47], v[80:83], v[144:147], v[32:47]\n"
    "v_mfma_f32_32x32x16_bf16 v[48:63], v[84:87], v[144:147], v[48:63]\n"
    "ds_read_b64_tr_b16 v[80:81], v141 offset:32768\n"
    "ds_read_b64_tr_b16 v[82:83], v141 offset:33792\n"
    "ds_read_b64_tr_b16 v[84:85], v2 offset:32768\n"
    "ds_read_b64_tr_b16 v[86:87], v2 offset:33792\n"
    "s_waitcnt vmcnt(5) lgkmcnt(14)\n"
    "v_mfma_f32_32x32x16_bf16 v[32:47], v[88:91], v[148:151], v[32:47]\n"
    "v_mfma_f32_32x32x16_bf16 v[48:63], v[92:95], v[148:151], v[48:63]\n"
    "buffer_load_dwordx4 v[112:115], v0, s[16:19], s39 offen offset:2048 sc1\n"
    "s_add_u32 s69, s39, 4096\n"
    "buffer_load_dwordx4 v[132:135], v0, s[16:19], s69 offen offset:3072 sc1\n"
    "ds_read_b64_tr_b16 v[88:89], v141 offset:40960\n"
    "ds_read_b64_tr_b16 v[90:91], v141 offset:41984\n"
    "ds_read_b64_tr_b16 v[92:93], v2 offset:40960\n"
    "ds_read_b64_tr_b16 v[94:95], v2 offset:41984\n"
    "s_waitcnt vmcnt(8) lgkmcnt(8)\n"
    "v_mfma_f32_32x32x16_bf16 v[32:47], v[96:99], v[152:155], v[32:47]\n"
    "v_mfma_f32_32x32x16_bf16 v[48:63], v[100:103], v[152:155], v[48:63]\n"
    "s_setprio 1\n"
    "buffer_load_dwordx4 v[116:119], v0, s[16:19], s39 offen offset:3072 sc1\n"
    "ds_read_b64_tr_b16 v[96:97], v141 offset:49152\n"
    "ds_read_b64_tr_b16 v[98:99], v141 offset:50176\n"
    "ds_read_b64_tr_b16 v[100:101], v2 offset:49152\n"
    "ds_read_b64_tr_b16 v[102:103], v2 offset:50176\n"
    "s_waitcnt vmcnt(7) lgkmcnt(8)\n"
    "v_mfma_f32_32x32x16_bf16 v[32:47], v[80:83], v[156:159], v[32:47]\n"
    "v_mfma_f32_32x32x16_bf16 v[48:63], v[84:87], v[156:159], v[48:63]\n"
    "buffer_load_dwordx4 v[120:123], v0, s[16:19], s69 offen offset:0 sc1\n"
    "s_waitcnt vmcnt(27) lgkmcnt(0)\n"
    "s_barrier\n"
    "ds_read_b64_tr_b16 v[80:81], v13 offset:0\n"
    "ds_read_b64_tr_b16 v[82:83], v13 offset:512\n"
    "ds_read_b64_tr_b16 v[84:85], v15 offset:0\n"
    "ds_read_b64_tr_b16 v[86:87], v15 offset:512\n"
    "s_waitcnt vmcnt(13) lgkmcnt(8)\n"
    "v_mfma_f32_32x32x16_bf16 v[32:47], v[88:91], v[160:163], v[32:47]\n"
    "v_mfma_f32_32x32x16_bf16 v[48:63], v[92:95], v[160:163], v[48:63]\n"
    "buffer_load_dwordx4 v[124:127], v0, s[16:19], s69 offen offset:1024 sc1\n"
    "buffer_load_dwordx4 v[128:131], v0, s[16:19], s69 offen offset:2048 sc1\n"
    "s_setprio 0\n"
    "ds_read_b64_tr_b16 v[88:89], v15 offset:1792\n"
    "ds_read_b64_tr_b16 v[90:91], v15 offset:2304\n"
    "ds_read_b64_tr_b16 v[92:93], v13 offset:1792\n"
    "ds_read_b64_tr_b16 v[94:95], v13 offset:2304\n"
    "s_waitcnt vmcnt(16) lgkmcnt(8)\n"
    "v_mfma_f32_32x32x16_bf16 v[32:47], v[96:99], v[164:167], v[32:47]\n"
    "v_mfma_f32_32x32x16_bf16 v[48:63], v[100:103], v[164:167], v[48:63]\n"
    "s_setprio 1\n"
    "ds_read_b64_tr_b16 v[96:97], v13 offset:3584\n"
    "ds_read_b64_tr_b16 v[98:99], v13 offset:4096\n"
    "ds_read_b64_tr_b16 v[100:101], v15 offset:3584\n"
    "ds_read_b64_tr_b16 v[102:103], v15 offset:4096\n"
    "s_waitcnt vmcnt(9) lgkmcnt(8)\n"
    "v_xor_b32 v171, s47, v171\n"
    "v_cmp_lt_u32 vcc, 0xffff, v171\n"
    "s_cbranch_vccnz .Lspin14_%=\n"
    ".Lok14_%=:\n"
    "v_mfma_f32_32x32x16_bf16 v[32:47], v[80:83], v[168:171], v[32:47]\n"
    "v_mfma_f32_32x32x16_bf16 v[48:63], v[84:87], v[168:171], v[48:63]\n"
    "ds_read_b64_tr_b16 v[80:81], v15 offset:5376\n"
    "ds_read_b64_tr_b16 v[82:83], v15 offset:5888\n"
    "ds_read_b64_tr_b16 v[84:85], v13 offset:5376\n"
    "ds_read_b64_tr_b16 v[86:87], v13 offset:5888\n"
    "s_waitcnt vmcnt(8) lgkmcnt(8)\n"
    "v_xor_b32 v175, s47, v175\n"
    "v_cmp_lt_u32 vcc, 0xffff, v175\n"
    "s_cbranch_vccnz .Lspin15_%=\n"
    ".Lok15_%=:\n"
    "v_mfma_f32_32x32x16_bf16 v[32:47], v[88:91], v[172:175], v[32:47]\n"
    "v_mfma_f32_32x32x16_bf16 v[48:63], v[92:95], v[172:175], v[48:63]\n"
    "ds_read_b64_tr_b16 v[88:89], v13 offset:7168\n"
    "ds_read_b64_tr_b16 v[90:91], v13 offset:7680\n"
    "ds_read_b64_tr_b16 v[92:93], v15 offset:7168\n"
    "ds_read_b64_tr_b16 v[94:95], v15 offset:7680\n"
    "s_waitcnt vmcnt(5) lgkmcnt(8)\n"
    "v_xor_b32 v115, s47, v115\n"
    "v_cmp_lt_u32 vcc, 0xffff, v115\n"
    "s_cbranch_vccnz .Lspin16_%=\n"
    ".Lok16_%=:\n"
    "v_mfma_f32_32x32x16_bf16 v[32:47], v[96:99], v[112:115], v[32:47]\n"
    "v_mfma_f32_32x32x16_bf16 v[48:63], v[100:103], v[112:115], v[48:63]\n"
    "ds_read_b64_tr_b16 v[96:97], v15 offset:8960\n"
    "ds_read_b64_tr_b16 v[98:99], v15 offset:9472\n"
    "ds_read_b64_tr_b16 v[100:101], v13 offset:8960\n"
    "ds_read_b64_tr_b16 v[102:103], v13 offset:9472\n"
    "s_waitcnt vmcnt(3) lgkmcnt(8)\n"
    "v_xor_b32 v119, s47, v119\n"
    "v_cmp_lt_u32 vcc, 0xffff, v119\n"
    "s_cbranch_vccnz .Lspin17_%=\n"
    ".Lok17_%=:\n"
    "v_mfma_f32_32x32x16_bf16 v[32:47], v[80:83], v[116:119], v[32:47]\n"
    "v_mfma_f32_32x32x16_bf16 v[48:63], v[84:87], v[116:119], v[48:63]\n"
    "buffer_load_dword v12, v111, s[16:19], s40 offen sc1\n"
    "buffer_load_dwordx4 v[184:187], v110, s[16:19], s44 offen offset:0 sc1\n"
    "buffer_load_dwordx4 v[188:191], v110, s[16:19], s44 offen offset:512 sc1\n"
    "s_setprio 0\n"
    "ds_read_b64_tr_b16 v[80:81], v13 offset:10752\n"
    "ds_read_b64_tr_b16 v[82:83], v13 offset:11264\n"
    "ds_read_b64_tr_b16 v[84:85], v15 offset:10752\n"
    "ds_read_b64_tr_b16 v[86:87], v15 offset:11264\n"
    "s_waitcnt vmcnt(5) lgkmcnt(8)\n"
    "v_xor_b32 v123, s47, v123\n"
    "v_cmp_lt_u32 vcc, 0xffff, v123\n"
    "s_cbranch_vccnz .Lspin18_%=\n"
    ".Lok18_%=:\n"
    "v_mfma_f32_32x32x16_bf16 v[32:47], v[88:91], v[120:123], v[32:47]\n"
    "v_mfma_f32_32x32x16_bf16 v[48:63], v[92:95], v[120:123], v[48:63]\n"
    "s_setprio 1\n"
    "ds_read_b64_tr_b16 v[88:89], v15 offset:12544\n"
    "ds_read_b64_tr_b16 v[90:91], v6 offset:13056\n"
    "ds_read_b64_tr_b16 v[92:93], v13 offset:12544\n"
    "ds_read_b64_tr_b16 v[94:95], v21 offset:13056\n"
    "s_waitcnt vmcnt(4) lgkmcnt(8)\n"
    "v_xor_b32 v127, s47, v127\n"
    "v_cmp_lt_u32 vcc, 0xffff, v127\n"
    "s_cbranch_vccnz .Lspin19_%=\n"
    ".Lok19_%=:\n"
    "v_mfma_f32_32x32x16_bf16 v[32:47], v[96:99], v[124:127], v[32:47]\n"
    "v_mfma_f32_32x32x16_bf16 v[48:63], v[100:103], v[124:127], v[48:63]\n"
    "s_waitcnt vmcnt(3) lgkmcnt(4)\n"
    "v_xor_b32 v131, s47, v131\n"
    "v_cmp_lt_u32 vcc, 0xffff, v131\n"
    "s_cbranch_vccnz .Lspin20_%=\n"
    ".Lok20_%=:\n"
    "v_mfma_f32_32x32x16_bf16 v[32:47], v[80:83], v[128:131], v[32:47]\n"
    "v_mfma_f32_32x32x16_bf16 v[48:63], v[84:87], v[128:131], v[48:63]\n"
    "s_waitcnt vmcnt(7) lgkmcnt(0)\n"
    "v_xor_b32 v135, s47, v135\n"
    "v_cmp_lt_u32 vcc, 0xffff, v135\n"
    "s_cbranch_vccnz .Lspin21_%=\n"
    ".Lok21_%=:\n"
    "v_mfma_f32_32x32x16_bf16 v[32:47], v[88:91], v[132:135], v[32:47]\n"
    "v_mfma_f32_32x32x16_bf16 v[48:63], v[92:95], v[132:135], v[48:63]\n"
    "s_setprio 0\n"
    ".Lfin_%=:\n"
    "s_nop 9\n"
    "ds_write_b128 v104, v[32:35] offset:0\n"
    "ds_write_b128 v104, v[36:39] offset:1088\n"
    "ds_write_b128 v105, v[40:43] offset:0\n"
    "ds_write_b128 v105, v[44:47] offset:1088\n"
    "ds_write_b128 v106, v[48:51] offset:0\n"
    "ds_write_b128 v106, v[52:55] offset:1088\n"
    "ds_write_b128 v107, v[56:59] offset:0\n"
    "ds_write_b128 v107, v[60:63] offset:1088\n"
    "s_waitcnt vmcnt(0)\n"
    "v_xor_b32 v184, s12, v184\n"
    "v_xor_b32 v185, s13, v185\n"
    "v_xor_b32 v186, s14, v186\n"
    "v_xor_b32 v187, s15, v187\n"
    "v_xor_b32 v188, s12, v188\n"
    "v_xor_b32 v189, s13, v189\n"
    "v_xor_b32 v190, s14, v190\n"
    "v_xor_b32 v191, s15, v191\n"
    "v_or3_b32 v25, v184, v185, v186\n"
    "v_or3_b32 v26, v187, v188, v189\n"
    "v_or3_b32 v25, v25, v190, v191\n"
    "v_xor_b32 v12, s12, v12\n"
    "v_or3_b32 v25, v25, v26, v12\n"
    "v_and_b32 v25, 0xff, v25\n"
    "v_cmp_ne_u32 vcc, 0, v25\n"
    "s_cbranch_vccnz .Lspin22_%=\n"
    ".Lok22_%=:\n"
    "v_pk_add_f32 v[176:177], v[176:177], v[184:185]\n"
    "v_pk_add_f32 v[178:179], v[178:179], v[186:187]\n"
    "v_pk_add_f32 v[180:181], v[180:181], v[188:189]\n"
    "v_pk_add_f32 v[182:183], v[182:183], v[190:191]\n"
    "s_waitcnt lgkmcnt(0)\n"
    "s_barrier\n"
    "ds_read_b128 v[32:35], v108 offset:0\n"
    "ds_read_b128 v[36:39], v108 offset:512\n"
    "ds_read_b128 v[40:43], v108 offset:8704\n"
    "ds_read_b128 v[44:47], v108 offset:9216\n"
    "ds_read_b128 v[48:51], v108 offset:17408\n"
    "ds_read_b128 v[52:55], v108 offset:17920\n"
    "ds_read_b128 v[56:59], v108 offset:26112\n"
    "ds_read_b128 v[60:63], v108 offset:26624\n"
    "s_waitcnt lgkmcnt(6)\n"
    "v_pk_add_f32 v[176:177], v[176:177], v[32:33]\n"
    "v_pk_add_f32 v[178:179], v[178:179], v[34:35]\n"
    "v_pk_add_f32 v[180:181], v[180:181], v[36:37]\n"
    "v_pk_add_f32 v[182:183], v[182:183], v[38:39]\n"
    "s_waitcnt lgkmcnt(4)\n"
    "v_pk_add_f32 v[176:177], v[176:177], v[40:41]\n"
    "v_pk_add_f32 v[178:179], v[178:179], v[42:43]\n"
    "v_pk_add_f32 v[180:181], v[180:181], v[44:45]\n"
    "v_pk_add_f32 v[182:183], v[182:183], v[46:47]\n"
    "s_waitcnt lgkmcnt(2)\n"
    "v_pk_add_f32 v[176:177], v[176:177], v[48:49]\n"
    "v_pk_add_f32 v[178:179], v[178:179], v[50:51]\n"
    "v_pk_add_f32 v[180:181], v[180:181], v[52:53]\n"
    "v_pk_add_f32 v[182:183], v[182:183], v[54:55]\n"
    "s_waitcnt lgkmcnt(0)\n"
    "v_pk_add_f32 v[176:177], v[176:177], v[56:57]\n"
    "v_pk_add_f32 v[178:179], v[178:179], v[58:59]\n"
    "v_pk_add_f32 v[180:181], v[180:181], v[60:61]\n"
    "v_pk_add_f32 v[182:183], v[182:183], v[62:63]\n"
    "v_pk_mul_f32 v[176:177], v[176:177], v[12:13] op_sel_hi:[1,0]\n"
    "v_pk_mul_f32 v[178:179], v[178:179], v[12:13] op_sel_hi:[1,0]\n"
    "v_pk_mul_f32 v[180:181], v[180:181], v[12:13] op_sel_hi:[1,0]\n"
    "v_pk_mul_f32 v[182:183], v[182:183], v[12:13] op_sel_hi:[1,0]\n"
    "v_cvt_pk_bf16_f32 v112, v176, v177\n"
    "v_cvt_pk_bf16_f32 v113, v178, v179\n"
    "v_cvt_pk_bf16_f32 v114, v180, v181\n"
    "v_cvt_pk_bf16_f32 v115, v182, v183\n"
    "buffer_store_dwordx4 v[112:115], v109, s[28:31], s46 offen offset:0 sc1\n"
    "s_or_b32 s70, s2, s56\n"
    "s_cbranch_scc1 .Lend_%=\n"
    "buffer_store_dwordx4 v[16:19], v7, s[16:19], 0 offen offset:0 sc1\n"
    ".Lend_%=:\n"
    "s_endpgm\n"
    ".Lsp0_%=:\n"
    "s_setprio 3\n"
    "v_lshlrev_b32 v29, 7, v30\n"
    "v_cmp_gt_u32 vcc, 16, v30\n"
    "s_nop 1\n"
    "v_cndmask_b32 v20, v28, v29, vcc\n"
    "s_lshr_b32 s90, s60, 3\n"
    "s_lshl1_add_u32 s90, s59, s90\n"
    "s_lshl_b32 s91, s90, 11\n"
    "s_add_u32 s91, s91, 65536\n"
    "s_lshl_b32 s71, s90, 16\n"
    "s_add_u32 s71, s71, s65\n"
    "s_add_u32 s71, s71, 1048576\n"
    "s_mov_b32 s89, 32768\n"
    "v_and_b32 v22, 31, v30\n"
    "v_or_b32 v24, 2, v22\n"
    "v_cmp_eq_u32 s[0:1], 31, v24\n"
    "v_mul_u32_u24 v24, 37, v22\n"
    "v_lshrrev_b32 v24, 8, v24\n"
    "v_mul_u32_u24 v25, 7, v24\n"
    "v_sub_u32 v25, v22, v25\n"
    "v_cmp_lt_u32 vcc, 27, v22\n"
    "v_bfrev_b32 v6, v22\n"
    "v_lshrrev_b32 v6, 30, v6\n"
    "v_cndmask_b32 v24, v24, v6, vcc\n"
    "v_cndmask_b32 v25, v25, 7, vcc\n"
    "v_and_b32 v6, 2, v24\n"
    "v_and_b32 v24, 1, v24\n"
    "v_lshl_add_u32 v6, v6, 1, v24\n"
    "v_and_b32 v24, 4, v25\n"
    "v_add_u32 v25, v25, v24\n"
    "v_lshl_add_u32 v6, v6, 2, v25\n"
    "v_and_or_b32 v6, v30, 32, v6\n"
    "v_lshlrev_b32 v6, 4, v6\n"
    "v_mov_b32 v23, 0x3f803f80\n"
    "s_cmp_eq_u32 s58, 3\n"
    "s_cselect_b64 s[32:33], s[54:55], 0\n"
    "s_lshl_b32 s92, s90, 12\n"
    "s_add_u32 s92, s92, s65\n"
    "s_add_u32 s92, s92, 262144\n"
    "v_mov_b32 v113, s52\n"
    "v_mov_b32 v115, s52\n"
    "v_mov_b32 v25, 1\n"
    "s_barrier\n"
    "s_branch .Lrsum_%=\n"
    ".Lrall_%=:\n"
    "s_mov_b32 s86, 0xffff\n"
    ".Lrpoll_%=:\n"
    "buffer_load_dwordx2 v[26:27], v20, s[16:19], s91 offen sc1\n"
    "s_sub_u32 s89, s89, 1\n"
    "s_cbranch_scc1 .Lrdone_%=\n"
    "s_waitcnt vmcnt(0)\n"
    "v_cmp_eq_u64 vcc, s[52:53], v[26:27]\n"
    "s_nop 0\n"
    "s_and_b32 s70, vcc_lo, s86\n"
    "s_cbranch_scc0 .Lrpoll_%=\n"
    "s_bitcmp1_b32 s70, 0\n"
    "s_cbranch_scc0 .Lrd0_%=\n"
    "s_add_u32 s94, s71, 0\n"
    "buffer_load_dwordx4 v[144:147], v0, s[16:19], s94 offen sc1\n"
    ".Lrd0_%=:\n"
    "s_bitcmp1_b32 s70, 1\n"
    "s_cbranch_scc0 .Lrd1_%=\n"
    "s_add_u32 s94, s71, 4096\n"
    "buffer_load_dwordx4 v[148:151], v0, s[16:19], s94 offen sc1\n"
    ".Lrd1_%=:\n"
    "s_bitcmp1_b32 s70, 2\n"
    "s_cbranch_scc0 .Lrd2_%=\n"
    "s_add_u32 s94, s71, 8192\n"
    "buffer_load_dwordx4 v[152:155], v0, s[16:19], s94 offen sc1\n"
    ".Lrd2_%=:\n"
    "s_bitcmp1_b32 s70, 3\n"
    "s_cbranch_scc0 .Lrd3_%=\n"
    "s_add_u32 s94, s71, 12288\n"
    "buffer_load_dwordx4 v[156:159], v0, s[16:19], s94 offen sc1\n"
    ".Lrd3_%=:\n"
    "s_bitcmp1_b32 s70, 4\n"
    "s_cbranch_scc0 .Lrd4_%=\n"
    "s_add_u32 s94, s71, 16384\n"
    "buffer_load_dwordx4 v[160:163], v0, s[16:19], s94 offen sc1\n"
    ".Lrd4_%=:\n"
    "s_bitcmp1_b32 s70, 5\n"
    "s_cbranch_scc0 .Lrd5_%=\n"
    "s_add_u32 s94, s71, 20480\n"
    "buffer_load_dwordx4 v[164:167], v0, s[16:19], s94 offen sc1\n"
    ".Lrd5_%=:\n"
    "s_bitcmp1_b32 s70, 6\n"
    "s_cbranch_scc0 .Lrd6_%=\n"
    "s_add_u32 s94, s71, 24576\n"
    "buffer_load_dwordx4 v[168:171], v0, s[16:19], s94 offen sc1\n"
    ".Lrd6_%=:\n"
    "s_bitcmp1_b32 s70, 7\n"
    "s_cbranch_scc0 .Lrd7_%=\n"
    "s_add_u32 s94, s71, 28672\n"
    "buffer_load_dwordx4 v[172:175], v0, s[16:19], s94 offen sc1\n"
    ".Lrd7_%=:\n"
    "s_bitcmp1_b32 s70, 8\n"
    "s_cbranch_scc0 .Lrd8_%=\n"
    "s_add_u32 s94, s71, 32768\n"
    "buffer_load_dwordx4 v[176:179], v0, s[16:19], s94 offen sc1\n"
    ".Lrd8_%=:\n"
    "s_bitcmp1_b32 s70, 9\n"
    "s_cbranch_scc0 .Lrd9_%=\n"
    "s_add_u32 s94, s71, 36864\n"
    "buffer_load_dwordx4 v[180:183], v0, s[16:19], s94 offen sc1\n"
    ".Lrd9_%=:\n"
    "s_bitcmp1_b32 s70, 10\n"
    "s_cbranch_scc0 .Lrd10_%=\n"
    "s_add_u32 s94, s71, 40960\n"
    "buffer_load_dwordx4 v[184:187], v0, s[16:19], s94 offen sc1\n"
    ".Lrd10_%=:\n"
    "s_bitcmp1_b32 s70, 11\n"
    "s_cbranch_scc0 .Lrd11_%=\n"
    "s_add_u32 s94, s71, 45056\n"
    "buffer_load_dwordx4 v[188:191], v0, s[16:19], s94 offen sc1\n"
    ".Lrd11_%=:\n"
    "s_bitcmp1_b32 s70, 12\n"
    "s_cbranch_scc0 .Lrd12_%=\n"
    "s_add_u32 s94, s71, 49152\n"
    "buffer_load_dwordx4 v[192:195], v0, s[16:19], s94 offen sc1\n"
    ".Lrd12_%=:\n"
    "s_bitcmp1_b32 s70, 13\n"
    "s_cbranch_scc0 .Lrd13_%=\n"
    "s_add_u32 s94, s71, 53248\n"
    "buffer_load_dwordx4 v[196:199], v0, s[16:19], s94 offen sc1\n"
    ".Lrd13_%=:\n"
    "s_bitcmp1_b32 s70, 14\n"
    "s_cbranch_scc0 .Lrd14_%=\n"
    "s_add_u32 s94, s71, 57344\n"
    "buffer_load_dwordx4 v[200:203], v0, s[16:19], s94 offen sc1\n"
    ".Lrd14_%=:\n"
    "s_bitcmp1_b32 s70, 15\n"
    "s_cbranch_scc0 .Lrd15_%=\n"
    "s_add_u32 s94, s71, 61440\n"
    "buffer_load_dwordx4 v[204:207], v0, s[16:19], s94 offen sc1\n"
    ".Lrd15_%=:\n"
    "s_andn2_b32 s86, s86, s70\n"
    "s_cbranch_scc1 .Lrpoll_%=\n"
    "v_mov_b32 v25, 0\n"
    "s_waitcnt vmcnt(0)\n"
    ".Lrsum_%=:\n"
    "v_xor_b32 v144, s12, v144\n"
    "v_xor_b32 v145, s13, v145\n"
    "v_xor_b32 v146, s14, v146\n"
    "v_xor_b32 v147, s15, v147\n"
    "v_or3_b32 v25, v25, v144, v145\n"
    "v_or3_b32 v25, v25, v146, v147\n"
    "v_pk_add_f32 v[80:81], 0, v[144:145]\n"
    "v_pk_add_f32 v[82:83], 0, v[146:147]\n"
    "v_xor_b32 v148, s12, v148\n"
    "v_xor_b32 v149, s13, v149\n"
    "v_xor_b32 v150, s14, v150\n"
    "v_xor_b32 v151, s15, v151\n"
    "v_or3_b32 v25, v25, v148, v149\n"
    "v_or3_b32 v25, v25, v150, v151\n"
    "v_pk_add_f32 v[80:81], v[80:81], v[148:149]\n"
    "v_pk_add_f32 v[82:83], v[82:83], v[150:151]\n"
    "v_xor_b32 v152, s12, v152\n"
    "v_xor_b32 v153, s13, v153\n"
    "v_xor_b32 v154, s14, v154\n"
    "v_xor_b32 v155, s15, v155\n"
    "v_or3_b32 v25, v25, v152, v153\n"
    "v_or3_b32 v25, v25, v154, v155\n"
    "v_pk_add_f32 v[80:81], v[80:81], v[152:153]\n"
    "v_pk_add_f32 v[82:83], v[82:83], v[154:155]\n"
    "v_xor_b32 v156, s12, v156\n"
    "v_xor_b32 v157, s13, v157\n"
    "v_xor_b32 v158, s14, v158\n"
    "v_xor_b32 v159, s15, v159\n"
    "v_or3_b32 v25, v25, v156, v157\n"
    "v_or3_b32 v25, v25, v158, v159\n"
    "v_pk_add_f32 v[80:81], v[80:81], v[156:157]\n"
    "v_pk_add_f32 v[82:83], v[82:83], v[158:159]\n"
    "v_xor_b32 v160, s12, v160\n"
    "v_xor_b32 v161, s13, v161\n"
    "v_xor_b32 v162, s14, v162\n"
    "v_xor_b32 v163, s15, v163\n"
    "v_or3_b32 v25, v25, v160, v161\n"
    "v_or3_b32 v25, v25, v162, v163\n"
    "v_pk_add_f32 v[80:81], v[80:81], v[160:161]\n"
    "v_pk_add_f32 v[82:83], v[82:83], v[162:163]\n"
    "v_xor_b32 v164, s12, v164\n"
    "v_xor_b32 v165, s13, v165\n"
    "v_xor_b32 v166, s14, v166\n"
    "v_xor_b32 v167, s15, v167\n"
    "v_or3_b32 v25, v25, v164, v165\n"
    "v_or3_b32 v25, v25, v166, v167\n"
    "v_pk_add_f32 v[80:81], v[80:81], v[164:165]\n"
    "v_pk_add_f32 v[82:83], v[82:83], v[166:167]\n"
    "v_xor_b32 v168, s12, v168\n"
    "v_xor_b32 v169, s13, v169\n"
    "v_xor_b32 v170, s14, v170\n"
    "v_xor_b32 v171, s15, v171\n"
    "v_or3_b32 v25, v25, v168, v169\n"
    "v_or3_b32 v25, v25, v170, v171\n"
    "v_pk_add_f32 v[80:81], v[80:81], v[168:169]\n"
    "v_pk_add_f32 v[82:83], v[82:83], v[170:171]\n"
    "v_xor_b32 v172, s12, v172\n"
    "v_xor_b32 v173, s13, v173\n"
    "v_xor_b32 v174, s14, v174\n"
    "v_xor_b32 v175, s15, v175\n"
    "v_or3_b32 v25, v25, v172, v173\n"
    "v_or3_b32 v25, v25, v174, v175\n"
    "v_pk_add_f32 v[80:81], v[80:81], v[172:173]\n"
    "v_pk_add_f32 v[82:83], v[82:83], v[174:175]\n"
    "v_xor_b32 v176, s12, v176\n"
    "v_xor_b32 v177, s13, v177\n"
    "v_xor_b32 v178, s14, v178\n"
    "v_xor_b32 v179, s15, v179\n"
    "v_or3_b32 v25, v25, v176, v177\n"
    "v_or3_b32 v25, v25, v178, v179\n"
    "v_pk_add_f32 v[80:81], v[80:81], v[176:177]\n"
    "v_pk_add_f32 v[82:83], v[82:83], v[178:179]\n"
    "v_xor_b32 v180, s12, v180\n"
    "v_xor_b32 v181, s13, v181\n"
    "v_xor_b32 v182, s14, v182\n"
    "v_xor_b32 v183, s15, v183\n"
    "v_or3_b32 v25, v25, v180, v181\n"
    "v_or3_b32 v25, v25, v182, v183\n"
    "v_pk_add_f32 v[80:81], v[80:81], v[180:181]\n"
    "v_pk_add_f32 v[82:83], v[82:83], v[182:183]\n"
    "v_xor_b32 v184, s12, v184\n"
    "v_xor_b32 v185, s13, v185\n"
    "v_xor_b32 v186, s14, v186\n"
    "v_xor_b32 v187, s15, v187\n"
    "v_or3_b32 v25, v25, v184, v185\n"
    "v_or3_b32 v25, v25, v186, v187\n"
    "v_pk_add_f32 v[80:81], v[80:81], v[184:185]\n"
    "v_pk_add_f32 v[82:83], v[82:83], v[186:187]\n"
    "v_xor_b32 v188, s12, v188\n"
    "v_xor_b32 v189, s13, v189\n"
    "v_xor_b32 v190, s14, v190\n"
    "v_xor_b32 v191, s15, v191\n"
    "v_or3_b32 v25, v25, v188, v189\n"
    "v_or3_b32 v25, v25, v190, v191\n"
    "v_pk_add_f32 v[80:81], v[80:81], v[188:189]\n"
    "v_pk_add_f32 v[82:83], v[82:83], v[190:191]\n"
    "v_xor_b32 v192, s12, v192\n"
    "v_xor_b32 v193, s13, v193\n"
    "v_xor_b32 v194, s14, v194\n"
    "v_xor_b32 v195, s15, v195\n"
    "v_or3_b32 v25, v25, v192, v193\n"
    "v_or3_b32 v25, v25, v194, v195\n"
    "v_pk_add_f32 v[80:81], v[80:81], v[192:193]\n"
    "v_pk_add_f32 v[82:83], v[82:83], v[194:195]\n"
    "v_xor_b32 v196, s12, v196\n"
    "v_xor_b32 v197, s13, v197\n"
    "v_xor_b32 v198, s14, v198\n"
    "v_xor_b32 v199, s15, v199\n"
    "v_or3_b32 v25, v25, v196, v197\n"
    "v_or3_b32 v25, v25, v198, v199\n"
    "v_pk_add_f32 v[80:81], v[80:81], v[196:197]\n"
    "v_pk_add_f32 v[82:83], v[82:83], v[198:199]\n"
    "v_xor_b32 v200, s12, v200\n"
    "v_xor_b32 v201, s13, v201\n"
    "v_xor_b32 v202, s14, v202\n"
    "v_xor_b32 v203, s15, v203\n"
    "v_or3_b32 v25, v25, v200, v201\n"
    "v_or3_b32 v25, v25, v202, v203\n"
    "v_pk_add_f32 v[80:81], v[80:81], v[200:201]\n"
    "v_pk_add_f32 v[82:83], v[82:83], v[202:203]\n"
    "v_xor_b32 v204, s12, v204\n"
    "v_xor_b32 v205, s13, v205\n"
    "v_xor_b32 v206, s14, v206\n"
    "v_xor_b32 v207, s15, v207\n"
    "v_or3_b32 v25, v25, v204, v205\n"
    "v_or3_b32 v25, v25, v206, v207\n"
    "v_pk_add_f32 v[80:81], v[80:81], v[204:205]\n"
    "v_pk_add_f32 v[82:83], v[82:83], v[206:207]\n"
    "v_and_b32 v25, 0xff, v25\n"
    "v_cmp_ne_u32 vcc, 0, v25\n"
    "v_cvt_pk_bf16_f32 v112, v80, v81\n"
    "v_cndmask_b32 v112, v112, v23, s[0:1]\n"
    "v_cndmask_b32 v112, v112, 0, s[32:33]\n"
    "v_cvt_pk_bf16_f32 v114, v82, v83\n"
    "v_cndmask_b32 v114, v114, v23, s[0:1]\n"
    "v_cndmask_b32 v114, v114, 0, s[32:33]\n"
    "s_cmp_lg_u64 vcc, 0\n"
    "s_cbranch_scc1 .Lrall_%=\n"
    ".Lrdone_%=:\n"
    "s_waitcnt vmcnt(0)\n"
    "buffer_store_dwordx4 v[112:115], v6, s[16:19], s92 offen offset:0 sc1\n"
    "s_waitcnt vmcnt(0)\n"
    "s_barrier\n"
    "s_branch .Lend_%=\n"
    ".Lsp1_%=:\n"
    "s_waitcnt lgkmcnt(0)\n"
    "s_barrier\n"
    "s_add_u32 m0, s93, 147456\n"
    "s_add_u32 s94, s92, 16384\n"
    "buffer_load_dwordx4 v0, s[16:19], s94 offen sc1 lds\n"
    "s_add_u32 m0, s93, 151552\n"
    "s_add_u32 s94, s92, 20480\n"
    "buffer_load_dwordx4 v0, s[16:19], s94 offen sc1 lds\n"
    "s_add_u32 m0, s93, 155648\n"
    "s_add_u32 s94, s92, 24576\n"
    "buffer_load_dwordx4 v0, s[16:19], s94 offen sc1 lds\n"
    "s_waitcnt vmcnt(0)\n"
    "s_barrier\n"
    "s_branch .Lq_%=\n"
    ".Lsp1o_%=:\n"
    "s_waitcnt vmcnt(0)\n"
    "s_barrier\n"
    "s_mov_b32 s89, 32768\n"
    "s_add_u32 s94, s42, 4096\n"
    ".Lkfl_%=:\n"
    "buffer_load_dwordx4 v[144:147], v0, s[16:19], s42 offen offset:0 sc1\n"
    "buffer_load_dwordx4 v[148:151], v0, s[16:19], s42 offen offset:1024 sc1\n"
    "buffer_load_dwordx4 v[152:155], v0, s[16:19], s42 offen offset:2048 sc1\n"
    "buffer_load_dwordx4 v[156:159], v0, s[16:19], s42 offen offset:3072 sc1\n"
    "buffer_load_dwordx4 v[160:163], v0, s[16:19], s94 offen offset:0 sc1\n"
    "buffer_load_dwordx4 v[164:167], v0, s[16:19], s94 offen offset:1024 sc1\n"
    "buffer_load_dwordx4 v[168:171], v0, s[16:19], s94 offen offset:2048 sc1\n"
    "buffer_load_dwordx4 v[172:175], v0, s[16:19], s94 offen offset:3072 sc1\n"
    "s_sub_u32 s89, s89, 1\n"
    "s_waitcnt vmcnt(0)\n"
    "s_cbranch_scc1 .Lkfd_%=\n"
    "v_mov_b32 v22, 0\n"
    "v_bitop3_b32 v22, v22, v145, s52 bitop3:0xf6\n"
    "v_bitop3_b32 v22, v22, v147, s52 bitop3:0xf6\n"
    "v_bitop3_b32 v22, v22, v149, s52 bitop3:0xf6\n"
    "v_bitop3_b32 v22, v22, v151, s52 bitop3:0xf6\n"
    "v_bitop3_b32 v22, v22, v153, s52 bitop3:0xf6\n"
    "v_bitop3_b32 v22, v22, v155, s52 bitop3:0xf6\n"
    "v_bitop3_b32 v22, v22, v157, s52 bitop3:0xf6\n"
    "v_bitop3_b32 v22, v22, v159, s52 bitop3:0xf6\n"
    "v_bitop3_b32 v22, v22, v161, s52 bitop3:0xf6\n"
    "v_bitop3_b32 v22, v22, v163, s52 bitop3:0xf6\n"
    "v_bitop3_b32 v22, v22, v165, s52 bitop3:0xf6\n"
    "v_bitop3_b32 v22, v22, v167, s52 bitop3:0xf6\n"
    "v_bitop3_b32 v22, v22, v169, s52 bitop3:0xf6\n"
    "v_bitop3_b32 v22, v22, v171, s52 bitop3:0xf6\n"
    "v_bitop3_b32 v22, v22, v173, s52 bitop3:0xf6\n"
    "v_bitop3_b32 v22, v22, v175, s52 bitop3:0xf6\n"
    "v_cmp_ne_u32 vcc, 0, v22\n"
    "s_nop 0\n"
    "s_cmp_lg_u64 vcc, 0\n"
    "s_cbranch_scc1 .Lkfl_%=\n"
    ".Lkfd_%=:\n"
    "v_mov_b32 v208, v144\n"
    "v_mov_b32 v209, v146\n"
    "v_mov_b32 v210, v148\n"
    "v_mov_b32 v211, v150\n"
    "v_mov_b32 v212, v152\n"
    "v_mov_b32 v213, v154\n"
    "v_mov_b32 v214, v156\n"
    "v_mov_b32 v215, v158\n"
    "v_mov_b32 v216, v160\n"
    "v_mov_b32 v217, v162\n"
    "v_mov_b32 v218, v164\n"
    "v_mov_b32 v219, v166\n"
    "v_mov_b32 v220, v168\n"
    "v_mov_b32 v221, v170\n"
    "v_mov_b32 v222, v172\n"
    "v_mov_b32 v223, v174\n"
    "v_mfma_f32_32x32x16_bf16 v[224:239], v[208:211], v[64:67], 0\n"
    "v_mfma_f32_32x32x16_bf16 v[240:255], v[216:219], v[72:75], 0\n"
    "v_mfma_f32_32x32x16_bf16 v[224:239], v[212:215], v[68:71], v[224:239]\n"
    "v_mfma_f32_32x32x16_bf16 v[240:255], v[220:223], v[76:79], v[240:255]\n"
    "s_nop 10\n"
    "v_rcp_f32 v22, v239\n"
    "v_rcp_f32 v23, v255\n"
    "v_fmaak_f32 v24, v231, v22, 0x358637bd\n"
    "v_fmac_f32 v24, v247, v23\n"
    "v_rcp_f32 v24, v24\n"
    "s_nop 0\n"
    "v_mul_f32 v24, v14, v24\n"
    "v_mul_f32 v22, v24, v22\n"
    "v_mul_f32 v23, v24, v23\n"
    "v_mul_f32 v224, v22, v224\n"
    "v_mul_f32 v225, v22, v225\n"
    "v_fmac_f32 v224, v23, v240\n"
    "v_fmac_f32 v225, v23, v241\n"
    "v_cvt_pk_bf16_f32 v208, v224, v225\n"
    "v_mul_f32 v226, v22, v226\n"
    "v_mul_f32 v227, v22, v227\n"
    "v_fmac_f32 v226, v23, v242\n"
    "v_fmac_f32 v227, v23, v243\n"
    "v_cvt_pk_bf16_f32 v209, v226, v227\n"
    "v_mul_f32 v228, v22, v228\n"
    "v_mul_f32 v229, v22, v229\n"
    "v_fmac_f32 v228, v23, v244\n"
    "v_fmac_f32 v229, v23, v245\n"
    "v_cvt_pk_bf16_f32 v210, v228, v229\n"
    "v_mul_f32 v230, v22, v230\n"
    "v_fmac_f32 v230, v23, v246\n"
    "v_cvt_pk_bf16_f32 v211, v230, 0\n"
    "v_or_b32 v211, s47, v211\n"
    "v_mul_f32 v232, v22, v232\n"
    "v_mul_f32 v233, v22, v233\n"
    "v_fmac_f32 v232, v23, v248\n"
    "v_fmac_f32 v233, v23, v249\n"
    "v_cvt_pk_bf16_f32 v212, v232, v233\n"
    "v_mul_f32 v234, v22, v234\n"
    "v_mul_f32 v235, v22, v235\n"
    "v_fmac_f32 v234, v23, v250\n"
    "v_fmac_f32 v235, v23, v251\n"
    "v_cvt_pk_bf16_f32 v213, v234, v235\n"
    "v_mul_f32 v236, v22, v236\n"
    "v_mul_f32 v237, v22, v237\n"
    "v_fmac_f32 v236, v23, v252\n"
    "v_fmac_f32 v237, v23, v253\n"
    "v_cvt_pk_bf16_f32 v214, v236, v237\n"
    "v_mul_f32 v238, v22, v238\n"
    "v_fmac_f32 v238, v23, v254\n"
    "v_cvt_pk_bf16_f32 v215, v238, 0\n"
    "v_or_b32 v215, s47, v215\n"
    "buffer_store_dwordx4 v[208:211], v0, s[16:19], s81 offen offset:0 sc1\n"
    "buffer_store_dwordx4 v[212:215], v0, s[16:19], s81 offen offset:1024 sc1\n"
    "s_branch .Lend_%=\n"
    ".Lspin0_%=:\n"
    "s_mov_b32 s89, 0x8000\n"
    ".Lspin0_l_%=:\n"
    "buffer_load_dwordx2 v[26:27], v20, s[16:19], s51 offen offset:0 sc1\n"
    "s_sub_u32 s89, s89, 1\n"
    "s_cbranch_scc1 .Lspin0_x_%=\n"
    "s_waitcnt vmcnt(0)\n"
    "v_cmp_eq_u64 vcc, s[52:53], v[26:27]\n"
    "s_nop 0\n"
    "s_andn2_b32 s70, -1, vcc_lo\n"
    "s_cbranch_scc1 .Lspin0_l_%=\n"
    ".Lspin0_x_%=:\n"
    "s_waitcnt vmcnt(0)\n"
    "s_branch .Lok0_%=\n"
    ".Lspin1_%=:\n"
    "s_mov_b32 s89, 0x8000\n"
    ".Lspin1_l_%=:\n"
    "buffer_load_dwordx2 v[26:27], v143, s[16:19], s38 offen offset:2048 sc1\n"
    "s_sub_u32 s89, s89, 1\n"
    "s_cbranch_scc1 .Lspin1_x_%=\n"
    "s_waitcnt vmcnt(0)\n"
    "v_cmp_eq_u64 vcc, s[52:53], v[26:27]\n"
    "s_nop 0\n"
    "s_andn2_b32 s70, 127, vcc_lo\n"
    "s_cbranch_scc1 .Lspin1_l_%=\n"
    ".Lspin1_x_%=:\n"
    "s_waitcnt vmcnt(0)\n"
    "s_branch .Lok1_%=\n"
    ".Lspin2_%=:\n"
    "s_mov_b32 s89, 0x8000\n"
    ".Lspin2_l_%=:\n"
    "buffer_load_dwordx4 v[208:211], v0, s[16:19], s41 offen sc1\n"
    "buffer_load_dwordx4 v[212:215], v0, s[16:19], s41 offen offset:1024 sc1\n"
    "s_sub_u32 s89, s89, 1\n"
    "s_waitcnt vmcnt(0)\n"
    "s_cbranch_scc1 .Lok2_%=\n"
    "v_xor_b32 v209, s52, v209\n"
    "v_bitop3_b32 v209, v209, v211, s52 bitop3:0xf6\n"
    "v_bitop3_b32 v209, v209, v213, s52 bitop3:0xf6\n"
    "v_bitop3_b32 v209, v209, v215, s52 bitop3:0xf6\n"
    "v_cmp_ne_u32 vcc, 0, v209\n"
    "s_cbranch_vccnz .Lspin2_l_%=\n"
    "s_branch .Lok2_%=\n"
    ".Lspin3_%=:\n"
    "s_mov_b32 s89, 0x8000\n"
    ".Lspin3_l_%=:\n"
    "buffer_load_dwordx2 v[26:27], v143, s[16:19], s38 offen offset:0 sc1\n"
    "s_sub_u32 s89, s89, 1\n"
    "s_cbranch_scc1 .Lspin3_x_%=\n"
    "s_waitcnt vmcnt(0)\n"
    "v_cmp_eq_u64 vcc, s[52:53], v[26:27]\n"
    "s_nop 0\n"
    "s_andn2_b32 s70, 127, vcc_lo\n"
    "s_cbranch_scc1 .Lspin3_l_%=\n"
    ".Lspin3_x_%=:\n"
    "s_waitcnt vmcnt(0)\n"
    "s_branch .Lok3_%=\n"
    ".Lspin4_%=:\n"
    "s_mov_b32 s89, 0x8000\n"
    "s_add_u32 s70, s39, 0\n"
    ".Lspin4_l_%=:\n"
    "buffer_load_dwordx4 v[168:171], v0, s[16:19], s70 offen sc1\n"
    "s_sub_u32 s89, s89, 1\n"
    "s_waitcnt vmcnt(0)\n"
    "s_cbranch_scc1 .Lok4_%=\n"
    "v_xor_b32 v171, s47, v171\n"
    "v_cmp_lt_u32 vcc, 0xffff, v171\n"
    "s_cbranch_vccnz .Lspin4_l_%=\n"
    "s_branch .Lok4_%=\n"
    ".Lspin5_%=:\n"
    "s_mov_b32 s89, 0x8000\n"
    "s_add_u32 s70, s39, 1024\n"
    ".Lspin5_l_%=:\n"
    "buffer_load_dwordx4 v[172:175], v0, s[16:19], s70 offen sc1\n"
    "s_sub_u32 s89, s89, 1\n"
    "s_waitcnt vmcnt(0)\n"
    "s_cbranch_scc1 .Lok5_%=\n"
    "v_xor_b32 v175, s47, v175\n"
    "v_cmp_lt_u32 vcc, 0xffff, v175\n"
    "s_cbranch_vccnz .Lspin5_l_%=\n"
    "s_branch .Lok5_%=\n"
    ".Lspin6_%=:\n"
    "s_mov_b32 s89, 0x8000\n"
    "s_add_u32 s70, s39, 2048\n"
    ".Lspin6_l_%=:\n"
    "buffer_load_dwordx4 v[112:115], v0, s[16:19], s70 offen sc1\n"
    "s_sub_u32 s89, s89, 1\n"
    "s_waitcnt vmcnt(0)\n"
    "s_cbranch_scc1 .Lok6_%=\n"
    "v_xor_b32 v115, s47, v115\n"
    "v_cmp_lt_u32 vcc, 0xffff, v115\n"
    "s_cbranch_vccnz .Lspin6_l_%=\n"
    "s_branch .Lok6_%=\n"
    ".Lspin7_%=:\n"
    "s_mov_b32 s89, 0x8000\n"
    "s_add_u32 s70, s39, 3072\n"
    ".Lspin7_l_%=:\n"
    "buffer_load_dwordx4 v[116:119], v0, s[16:19], s70 offen sc1\n"
    "s_sub_u32 s89, s89, 1\n"
    "s_waitcnt vmcnt(0)\n"
    "s_cbranch_scc1 .Lok7_%=\n"
    "v_xor_b32 v119, s47, v119\n"
    "v_cmp_lt_u32 vcc, 0xffff, v119\n"
    "s_cbranch_vccnz .Lspin7_l_%=\n"
    "s_branch .Lok7_%=\n"
    ".Lspin8_%=:\n"
    "s_mov_b32 s89, 0x8000\n"
    "s_add_u32 s70, s39, 4096\n"
    ".Lspin8_l_%=:\n"
    "buffer_load_dwordx4 v[120:123], v0, s[16:19], s70 offen sc1\n"
    "s_sub_u32 s89, s89, 1\n"
    "s_waitcnt vmcnt(0)\n"
    "s_cbranch_scc1 .Lok8_%=\n"
    "v_xor_b32 v123, s47, v123\n"
    "v_cmp_lt_u32 vcc, 0xffff, v123\n"
    "s_cbranch_vccnz .Lspin8_l_%=\n"
    "s_branch .Lok8_%=\n"
    ".Lspin9_%=:\n"
    "s_mov_b32 s89, 0x8000\n"
    "s_add_u32 s70, s39, 5120\n"
    ".Lspin9_l_%=:\n"
    "buffer_load_dwordx4 v[124:127], v0, s[16:19], s70 offen sc1\n"
    "s_sub_u32 s89, s89, 1\n"
    "s_waitcnt vmcnt(0)\n"
    "s_cbranch_scc1 .Lok9_%=\n"
    "v_xor_b32 v127, s47, v127\n"
    "v_cmp_lt_u32 vcc, 0xffff, v127\n"
    "s_cbranch_vccnz .Lspin9_l_%=\n"
    "s_branch .Lok9_%=\n"
    ".Lspin10_%=:\n"
    "s_mov_b32 s89, 0x8000\n"
    "s_add_u32 s70, s39, 6144\n"
    ".Lspin10_l_%=:\n"
    "buffer_load_dwordx4 v[128:131], v0, s[16:19], s70 offen sc1\n"
    "s_sub_u32 s89, s89, 1\n"
    "s_waitcnt vmcnt(0)\n"
    "s_cbranch_scc1 .Lok10_%=\n"
    "v_xor_b32 v131, s47, v131\n"
    "v_cmp_lt_u32 vcc, 0xffff, v131\n"
    "s_cbranch_vccnz .Lspin10_l_%=\n"
    "s_branch .Lok10_%=\n"
    ".Lspin11_%=:\n"
    "s_mov_b32 s89, 0x8000\n"
    "s_add_u32 s70, s39, 7168\n"
    ".Lspin11_l_%=:\n"
    "buffer_load_dwordx4 v[132:135], v0, s[16:19], s70 offen sc1\n"
    "s_sub_u32 s89, s89, 1\n"
    "s_waitcnt vmcnt(0)\n"
    "s_cbranch_scc1 .Lok11_%=\n"
    "v_xor_b32 v135, s47, v135\n"
    "v_cmp_lt_u32 vcc, 0xffff, v135\n"
    "s_cbranch_vccnz .Lspin11_l_%=\n"
    "s_branch .Lok11_%=\n"
    ".Lspin12_%=:\n"
    "s_mov_b32 s89, 0x8000\n"
    ".Lspin12_l_%=:\n"
    "buffer_load_dwordx2 v[26:27], v143, s[16:19], s38 offen offset:0 sc1\n"
    "s_sub_u32 s89, s89, 1\n"
    "s_cbranch_scc1 .Lspin12_x_%=\n"
    "s_waitcnt vmcnt(0)\n"
    "v_cmp_eq_u64 vcc, s[52:53], v[26:27]\n"
    "s_nop 0\n"
    "s_andn2_b32 s70, 127, vcc_lo\n"
    "s_cbranch_scc1 .Lspin12_l_%=\n"
    ".Lspin12_x_%=:\n"
    "s_waitcnt vmcnt(0)\n"
    "s_branch .Lok12_%=\n"
    ".Lspin13_%=:\n"
    "s_mov_b32 s89, 0x8000\n"
    ".Lspin13_l_%=:\n"
    "buffer_load_dwordx2 v[26:27], v143, s[16:19], s38 offen offset:2048 sc1\n"
    "s_sub_u32 s89, s89, 1\n"
    "s_cbranch_scc1 .Lspin13_x_%=\n"
    "s_waitcnt vmcnt(0)\n"
    "v_cmp_eq_u64 vcc, s[52:53], v[26:27]\n"
    "s_nop 0\n"
    "s_andn2_b32 s70, 127, vcc_lo\n"
    "s_cbranch_scc1 .Lspin13_l_%=\n"
    ".Lspin13_x_%=:\n"
    "s_waitcnt vmcnt(0)\n"
    "s_branch .Lok13_%=\n"
    ".Lspin14_%=:\n"
    "s_mov_b32 s89, 0x8000\n"
    "s_add_u32 s70, s39, 0\n"
    ".Lspin14_l_%=:\n"
    "buffer_load_dwordx4 v[168:171], v0, s[16:19], s70 offen sc1\n"
    "s_sub_u32 s89, s89, 1\n"
    "s_waitcnt vmcnt(0)\n"
    "s_cbranch_scc1 .Lok14_%=\n"
    "v_xor_b32 v171, s47, v171\n"
    "v_cmp_lt_u32 vcc, 0xffff, v171\n"
    "s_cbranch_vccnz .Lspin14_l_%=\n"
    "s_branch .Lok14_%=\n"
    ".Lspin15_%=:\n"
    "s_mov_b32 s89, 0x8000\n"
    "s_add_u32 s70, s39, 1024\n"
    ".Lspin15_l_%=:\n"
    "buffer_load_dwordx4 v[172:175], v0, s[16:19], s70 offen sc1\n"
    "s_sub_u32 s89, s89, 1\n"
    "s_waitcnt vmcnt(0)\n"
    "s_cbranch_scc1 .Lok15_%=\n"
    "v_xor_b32 v175, s47, v175\n"
    "v_cmp_lt_u32 vcc, 0xffff, v175\n"
    "s_cbranch_vccnz .Lspin15_l_%=\n"
    "s_branch .Lok15_%=\n"
    ".Lspin16_%=:\n"
    "s_mov_b32 s89, 0x8000\n"
    "s_add_u32 s70, s39, 2048\n"
    ".Lspin16_l_%=:\n"
    "buffer_load_dwordx4 v[112:115], v0, s[16:19], s70 offen sc1\n"
    "s_sub_u32 s89, s89, 1\n"
    "s_waitcnt vmcnt(0)\n"
    "s_cbranch_scc1 .Lok16_%=\n"
    "v_xor_b32 v115, s47, v115\n"
    "v_cmp_lt_u32 vcc, 0xffff, v115\n"
    "s_cbranch_vccnz .Lspin16_l_%=\n"
    "s_branch .Lok16_%=\n"
    ".Lspin17_%=:\n"
    "s_mov_b32 s89, 0x8000\n"
    "s_add_u32 s70, s39, 3072\n"
    ".Lspin17_l_%=:\n"
    "buffer_load_dwordx4 v[116:119], v0, s[16:19], s70 offen sc1\n"
    "s_sub_u32 s89, s89, 1\n"
    "s_waitcnt vmcnt(0)\n"
    "s_cbranch_scc1 .Lok17_%=\n"
    "v_xor_b32 v119, s47, v119\n"
    "v_cmp_lt_u32 vcc, 0xffff, v119\n"
    "s_cbranch_vccnz .Lspin17_l_%=\n"
    "s_branch .Lok17_%=\n"
    ".Lspin18_%=:\n"
    "s_mov_b32 s89, 0x8000\n"
    "s_add_u32 s70, s39, 4096\n"
    ".Lspin18_l_%=:\n"
    "buffer_load_dwordx4 v[120:123], v0, s[16:19], s70 offen sc1\n"
    "s_sub_u32 s89, s89, 1\n"
    "s_waitcnt vmcnt(0)\n"
    "s_cbranch_scc1 .Lok18_%=\n"
    "v_xor_b32 v123, s47, v123\n"
    "v_cmp_lt_u32 vcc, 0xffff, v123\n"
    "s_cbranch_vccnz .Lspin18_l_%=\n"
    "s_branch .Lok18_%=\n"
    ".Lspin19_%=:\n"
    "s_mov_b32 s89, 0x8000\n"
    "s_add_u32 s70, s39, 5120\n"
    ".Lspin19_l_%=:\n"
    "buffer_load_dwordx4 v[124:127], v0, s[16:19], s70 offen sc1\n"
    "s_sub_u32 s89, s89, 1\n"
    "s_waitcnt vmcnt(0)\n"
    "s_cbranch_scc1 .Lok19_%=\n"
    "v_xor_b32 v127, s47, v127\n"
    "v_cmp_lt_u32 vcc, 0xffff, v127\n"
    "s_cbranch_vccnz .Lspin19_l_%=\n"
    "s_branch .Lok19_%=\n"
    ".Lspin20_%=:\n"
    "s_mov_b32 s89, 0x8000\n"
    "s_add_u32 s70, s39, 6144\n"
    ".Lspin20_l_%=:\n"
    "buffer_load_dwordx4 v[128:131], v0, s[16:19], s70 offen sc1\n"
    "s_sub_u32 s89, s89, 1\n"
    "s_waitcnt vmcnt(0)\n"
    "s_cbranch_scc1 .Lok20_%=\n"
    "v_xor_b32 v131, s47, v131\n"
    "v_cmp_lt_u32 vcc, 0xffff, v131\n"
    "s_cbranch_vccnz .Lspin20_l_%=\n"
    "s_branch .Lok20_%=\n"
    ".Lspin21_%=:\n"
    "s_mov_b32 s89, 0x8000\n"
    "s_add_u32 s70, s39, 7168\n"
    ".Lspin21_l_%=:\n"
    "buffer_load_dwordx4 v[132:135], v0, s[16:19], s70 offen sc1\n"
    "s_sub_u32 s89, s89, 1\n"
    "s_waitcnt vmcnt(0)\n"
    "s_cbranch_scc1 .Lok21_%=\n"
    "v_xor_b32 v135, s47, v135\n"
    "v_cmp_lt_u32 vcc, 0xffff, v135\n"
    "s_cbranch_vccnz .Lspin21_l_%=\n"
    "s_branch .Lok21_%=\n"
    ".Lspin22_%=:\n"
    "s_mov_b32 s89, 0x8000\n"
    ".Lspin22_l_%=:\n"
    "buffer_load_dword v12, v111, s[16:19], s40 offen sc1\n"
    "buffer_load_dwordx4 v[184:187], v110, s[16:19], s44 offen sc1\n"
    "buffer_load_dwordx4 v[188:191], v110, s[16:19], s44 offen offset:512 sc1\n"
    "s_sub_u32 s89, s89, 1\n"
    "s_waitcnt vmcnt(0)\n"
    "s_cbranch_scc1 .Lok22_%=\n"
    "v_xor_b32 v184, s12, v184\n"
    "v_xor_b32 v185, s13, v185\n"
    "v_xor_b32 v186, s14, v186\n"
    "v_xor_b32 v187, s15, v187\n"
    "v_xor_b32 v188, s12, v188\n"
    "v_xor_b32 v189, s13, v189\n"
    "v_xor_b32 v190, s14, v190\n"
    "v_xor_b32 v191, s15, v191\n"
    "v_or3_b32 v25, v184, v185, v186\n"
    "v_or3_b32 v26, v187, v188, v189\n"
    "v_or3_b32 v25, v25, v190, v191\n"
    "v_xor_b32 v12, s12, v12\n"
    "v_or3_b32 v25, v25, v26, v12\n"
    "v_and_b32 v25, 0xff, v25\n"
    "v_cmp_ne_u32 vcc, 0, v25\n"
    "s_cbranch_vccnz .Lspin22_l_%=\n"
    "s_branch .Lok22_%=\n"
    ".Llast_%=:\n"
    : "+{v0}"(tid)
    : "{s[0:1]}"(__builtin_amdgcn_kernarg_segment_ptr()), "{s2}"(blockIdx.x), "{s3}"((unsigned)(size_t)(__attribute__((address_space(3))) char*)smem)
    : "v1", "v2", "v3", "v4", "v5", "v6", "v7", "v8", "v9", "v10", "v11", "v12", "v13", "v14", "v15", "v16", "v17", "v18", "v19", "v20", "v21", "v22", "v23", "v24", "v25", "v26", "v27", "v28", "v29", "v30", "v31", "v32", "v33", "v34", "v35", "v36", "v37", "v38", "v39", "v40", "v41", "v42", "v43", "v44", "v45", "v46", "v47", "v48", "v49", "v50", "v51", "v52", "v53", "v54", "v55", "v56", "v57", "v58", "v59", "v60", "v61", "v62", "v63", "v64", "v65", "v66", "v67", "v68", "v69", "v70", "v71", "v72", "v73", "v74", "v75", "v76", "v77", "v78", "v79", "v80", "v81", "v82", "v83", "v84", "v85", "v86", "v87", "v88", "v89", "v90", "v91", "v92", "v93", "v94", "v95", "v96", "v97", "v98", "v99", "v100", "v101", "v102", "v103", "v104", "v105", "v106", "v107", "v108", "v109", "v110", "v111", "v112", "v113", "v114", "v115", "v116", "v117", "v118", "v119", "v120", "v121", "v122", "v123", "v124", "v125", "v126", "v127", "v128", "v129", "v130", "v131", "v132", "v133", "v134", "v135", "v136", "v137", "v138", "v139", "v140", "v141", "v142", "v143", "v144", "v145", "v146", "v147", "v148", "v149", "v150", "v151", "v152", "v153", "v154", "v155", "v156", "v157", "v158", "v159", "v160", "v161", "v162", "v163", "v164", "v165", "v166", "v167", "v168", "v169", "v170", "v171", "v172", "v173", "v174", "v175", "v176", "v177", "v178", "v179", "v180", "v181", "v182", "v183", "v184", "v185", "v186", "v187", "v188", "v189", "v190", "v191", "v192", "v193", "v194", "v195", "v196", "v197", "v198", "v199", "v200", "v201", "v202", "v203", "v204", "v205", "v206", "v207", "v208", "v209", "v210", "v211", "v212", "v213", "v214", "v215", "v216", "v217", "v218", "v219", "v220", "v221", "v222", "v223", "v224", "v225", "v226", "v227", "v228", "v229", "v230", "v231", "v232", "v233", "v234", "v235", "v236", "v237", "v238", "v239", "v240", "v241", "v242", "v243", "v244", "v245", "v246", "v247", "v248", "v249", "v250", "v251", "v252", "v253", "v254", "v255", "s4", "s5", "s6", "s7", "s8", "s9", "s10", "s11", "s12", "s13", "s14", "s15", "s16", "s17", "s18", "s19", "s20", "s21", "s22", "s23", "s24", "s25", "s26", "s27", "s28", "s29", "s30", "s31", "s33", "s34", "s35", "s36", "s37", "s38", "s39", "s40", "s41", "s42", "s43", "s44", "s45", "s46", "s47", "s48", "s49", "s50", "s51", "s52", "s53", "s54", "s55", "s56", "s57", "s58", "s59", "s60", "s61", "s62", "s63", "s64", "s65", "s66", "s67", "s68", "s69", "s70", "s71", "s72", "s73", "s74", "s75", "s76", "s77", "s78", "s79", "s80", "s81", "s82", "s83", "s84", "s85", "s86", "s87", "s88", "s89", "s90", "s91", "s92", "s93", "s94", "s95", "s96", "s97", "s98", "s99", "vcc", "scc", "memory");
}

extern "C" void kernel_launch(void* const* d_in, const int* in_sizes, int n_in, void* d_out, int out_size, void* d_ws, size_t ws_size,
                              hipStream_t stream) {
  const unsigned F = 0x00020000u;
  fused_kernel<<<256, 512, 0, stream>>>(d_in[0], 2048u * 896u, F,
                                        d_in[5], 448u * 3584u, F,
                                        d_in[1], 448u * 2688u, F,
                                        d_ws, WS_BYTES, F,
                                        d_in[7], 1792u * 896u, F,
                                        d_in[3], 448u * 896u, F,
                                        d_out, 2048u * 896u, F,
                                        d_in[2], 25088u, F,
                                        d_in[9], 896u, F,
                                        d_in[6], 3584u, F,
                                        d_in[8], 896u, F);
}
```
